```python
import torch

def _softmax(x, axis=-1):
    return torch.softmax(x, dim=axis)
def _gelu(x):
    return torch.nn.functional.gelu(x, approximate="tanh")
def _silu(x):
    return torch.nn.functional.silu(x)
def _sigmoid(x):
    return torch.sigmoid(x)
def _layernorm(x, axis=-1, eps=1e-5):
    m = x.mean(dim=axis, keepdim=True)
    v = x.var(dim=axis, keepdim=True, unbiased=False)
    return (x - m) / torch.sqrt(v + eps)
def _scatter_add(tgt, idx, src, axis=0):
    return tgt.clone().index_add_(axis, idx, src)
def _segment_sum(src, seg, n):
    out = torch.zeros((n,) + src.shape[1:], dtype=src.dtype)
    return out.index_add_(0, seg, src)
def _pad(x, pw):
    flat = tuple(v for pair in reversed(pw) for v in pair)
    return torch.nn.functional.pad(x, flat)

kernel_name = 'concat_heads__linear_attn_block__softmax_full_mha__prompt'

def setup_inputs():

    _w = (lambda a: (lambda t: t.to(torch.bfloat16) if t.is_floating_point() else t)(torch.as_tensor(a)))
    def _A_setup():
        B, S, D = 1, 2048, 1024
        return {
            "x": _w((lambda *s: torch.randn(*s, dtype=torch.bfloat16) * 0.05)(B, S, D)),
            "wqkv": _w((lambda *s: torch.randn(*s, dtype=torch.bfloat16) * 1.6)(D, 3 * D)),
            "wo": _w((lambda *s: torch.randn(*s, dtype=torch.bfloat16) * 0.1)(D, D)),
        }
    _A_in = _A_setup()

    def _B_setup():
        B, S, D = 1, 2048, 1024
        return {
            "x": _w((lambda *s: torch.randn(*s, dtype=torch.bfloat16) * 0.2)(B, S, D)),
            "wqkv": _w((lambda *s: torch.randn(*s, dtype=torch.bfloat16) * 0.2)(D, 3 * D)),
            "wo": _w((lambda *s: torch.randn(*s, dtype=torch.bfloat16) * 0.05)(D, D)),
        }
    _B_in = _B_setup()

    return {
        "A_x": _A_in["x"],
        "A_wqkv": _A_in["wqkv"],
        "A_wo": _A_in["wo"],
        "B_x": _B_in["x"],
        "B_wqkv": _B_in["wqkv"],
        "B_wo": _B_in["wo"],
    }

def reference(A_x, A_wqkv, A_wo, B_x, B_wqkv, B_wo):
    def _A_ref(x, wqkv, wo):
        B, S, D = x.shape
        H = 32; Dh = D // H
        qkv = (lambda x, *d: x.permute(*d))((x @ wqkv).reshape(B, S, 3, H, Dh), 2, 0, 3, 1, 4)
        q, k, v = qkv[0], qkv[1], qkv[2]
        phi = lambda t: torch.where(t > 0, t + 1, torch.exp(t)) + 1e-6
        pq, pk = phi(q), phi(k)
        kv = torch.einsum("bhsd,bhse->bhde", pk, v)
        z = torch.einsum("bhsd->bhd", pk)
        num = torch.einsum("bhqd,bhde->bhqe", pq, kv)
        den = torch.einsum("bhqd,bhd->bhq", pq, z)[..., None] + 1e-6
        o = num / den
        return (lambda x, *d: x.permute(*d))(o, 0, 2, 1, 3).reshape(B, S, D) @ wo

    def _B_ref(x, wqkv, wo):
        B, S, D = x.shape
        H = 32; Dh = D // H
        qkv = (lambda x, *d: x.permute(*d))((x @ wqkv).reshape(B, S, 3, H, Dh), 2, 0, 3, 1, 4)
        q, k, v = qkv[0], qkv[1], qkv[2]
        scale = Dh ** -0.5
        scores = torch.einsum("bhqd,bhkd->bhqk", q, k) * scale
        p = _softmax(scores, axis=-1)
        o = torch.einsum("bhqk,bhkd->bhqd", p, v)
        return (lambda x, *d: x.permute(*d))(o, 0, 2, 1, 3).reshape(B, S, D) @ wo

    a = _A_ref(A_x, A_wqkv, A_wo)
    b = _B_ref(B_x, B_wqkv, B_wo)
    return torch.concatenate([a, b], axis=-1)
```

```cpp
#include <hip/hip_runtime.h>
#include <hip/hip_bf16.h>
typedef unsigned short u16;
__device__ __forceinline__ float bf2f(u16 v){ return __uint_as_float(((unsigned)v)<<16); }
__device__ __forceinline__ u16 f2bf(float f){ __bf16 b=(__bf16)f; return __builtin_bit_cast(u16,b); }
constexpr int S=2048, D=1024, H=32, DH=32;

__global__ void gemm_naive(const u16* A, const u16* B, u16* C, int M, int N, int K, int ldc){
  __shared__ float sa[16][64+1]; __shared__ float sb[16][64+1];
  int tx=threadIdx.x&15, ty=threadIdx.x>>4; int bm=blockIdx.y*64, bn=blockIdx.x*64;
  float acc[4][4]={};
  for(int k0=0;k0<K;k0+=16){
    for(int i=threadIdx.x;i<64*16;i+=256){ int r=i>>4,c=i&15; sa[c][r]=bf2f(A[(long)(bm+r)*K+k0+c]); }
    for(int i=threadIdx.x;i<64*16;i+=256){ int r=i>>6,c=i&63; sb[r][c]=bf2f(B[(long)(k0+r)*N+bn+c]); }
    __syncthreads();
    for(int k=0;k<16;k++){ float a[4],b[4]; for(int i=0;i<4;i++){a[i]=sa[k][ty*4+i]; b[i]=sb[k][tx*4+i];}
      for(int i=0;i<4;i++)for(int j=0;j<4;j++)acc[i][j]+=a[i]*b[j]; }
    __syncthreads();
  }
  for(int i=0;i<4;i++)for(int j=0;j<4;j++) C[(long)(bm+ty*4+i)*ldc+bn+tx*4+j]=f2bf(acc[i][j]);
}
__device__ __forceinline__ float phi(float t){ return (t>0.f? t+1.f : __expf(t))+1e-6f; }
__global__ void lin_kv(const u16* qkv, float* kv, float* z){
  int h=blockIdx.x; int t=threadIdx.x;
  int d=t>>5,e=t&31; float acc=0, zz=0;
  for(int s=0;s<S;s++){ float pk=phi(bf2f(qkv[(long)s*3072+1024+h*32+d])); float v=bf2f(qkv[(long)s*3072+2048+h*32+e]); acc+=pk*v; zz+=pk; }
  kv[h*1024+t]=acc; if(e==0) z[h*32+d]=zz;
}
__global__ void lin_o(const u16* qkv, const float* kv, const float* z, u16* o){
  int s=blockIdx.x; int t=threadIdx.x; int h=t>>5,e=t&31; float num=0,den=0;
  for(int d=0;d<32;d++){ float pq=phi(bf2f(qkv[(long)s*3072+h*32+d])); num+=pq*kv[h*1024+d*32+e]; den+=pq*z[h*32+d]; }
  o[(long)s*1024+t]=f2bf(num/(den+1e-6f));
}
__global__ void attn(const u16* qkv, u16* o){
  int h=blockIdx.y; int q=blockIdx.x*64+threadIdx.x;
  __shared__ float sk[64][33], sv[64][33];
  float qr[32], acc[32]; for(int d=0;d<32;d++){qr[d]=bf2f(qkv[(long)q*3072+h*32+d])*0.17677669529663687f; acc[d]=0;}
  float m=-1e30f,l=0;
  for(int k0=0;k0<S;k0+=64){
    __syncthreads();
    for(int i=threadIdx.x;i<64*32;i+=64){int r=i>>5,c=i&31; sk[r][c]=bf2f(qkv[(long)(k0+r)*3072+1024+h*32+c]); sv[r][c]=bf2f(qkv[(long)(k0+r)*3072+2048+h*32+c]);}
    __syncthreads();
    for(int k=0;k<64;k++){ float s=0; for(int d=0;d<32;d++) s+=qr[d]*sk[k][d]; float mn=fmaxf(m,s); float a=__expf(m-mn), p=__expf(s-mn); l=l*a+p; for(int d=0;d<32;d++) acc[d]=acc[d]*a+p*sv[k][d]; m=mn; }
  }
  for(int d=0;d<32;d++) o[(long)q*1024+h*32+d]=f2bf(acc[d]/l);
}
extern "C" void kernel_launch(void* const* d_in, const int* in_sizes, int n_in, void* d_out, int out_size, void* d_ws, size_t ws_size, hipStream_t stream){
  const u16* Ax=(const u16*)d_in[0]; const u16* Aw=(const u16*)d_in[1]; const u16* Ao=(const u16*)d_in[2];
  const u16* Bx=(const u16*)d_in[3]; const u16* Bw=(const u16*)d_in[4]; const u16* Bo=(const u16*)d_in[5];
  u16* out=(u16*)d_out; char* ws=(char*)d_ws;
  u16* qkvA=(u16*)ws; u16* qkvB=(u16*)(ws+(16<<20)); u16* oA=(u16*)(ws+(32<<20)); u16* oB=(u16*)(ws+(40<<20)); float* kv=(float*)(ws+(48<<20)); float* z=(float*)(ws+(49<<20));
  gemm_naive<<<dim3(48,32),256,0,stream>>>(Ax,Aw,qkvA,2048,3072,1024,3072);
  gemm_naive<<<dim3(48,32),256,0,stream>>>(Bx,Bw,qkvB,2048,3072,1024,3072);
  lin_kv<<<32,1024,0,stream>>>(qkvA,kv,z);
  lin_o<<<2048,1024,0,stream>>>(qkvA,kv,z,oA);
  attn<<<dim3(32,32),64,0,stream>>>(qkvB,oB);
  gemm_naive<<<dim3(16,32),256,0,stream>>>(oA,Ao,out,2048,1024,1024,2048);
  gemm_naive<<<dim3(16,32),256,0,stream>>>(oB,Bo,out+1024,2048,1024,1024,2048);
}
```

```cpp
#include <hip/hip_runtime.h>
#include <hip/hip_bf16.h>
typedef unsigned short u16;
typedef __bf16 bf16x8 __attribute__((ext_vector_type(8)));
typedef short s16x4 __attribute__((ext_vector_type(4)));
typedef float f32x16 __attribute__((ext_vector_type(16)));
typedef float f32x4 __attribute__((ext_vector_type(4)));
typedef float f32x2 __attribute__((ext_vector_type(2)));
typedef __bf16 bf16x2 __attribute__((ext_vector_type(2)));
typedef unsigned u32x4 __attribute__((ext_vector_type(4)));
typedef unsigned u32x2 __attribute__((ext_vector_type(2)));
#define LAS __attribute__((address_space(3)))
#define DI __device__ __forceinline__
#define SBAR() __builtin_amdgcn_sched_barrier(0)
#define MFMA32(a, b, c) __builtin_amdgcn_mfma_f32_32x32x16_bf16(a, b, c, 0, 0, 0)
#define MFMA16(a, b, c) __builtin_amdgcn_mfma_f32_16x16x32_bf16(a, b, c, 0, 0, 0)

constexpr int SEQ = 2048, DM = 1024, NH = 32;
constexpr size_t WS_QB = 0;
constexpr size_t WS_KV = WS_QB + (4u << 20);
constexpr size_t WS_PQ = WS_KV + (8u << 20);
constexpr size_t WS_OA = WS_PQ + (4u << 20);
constexpr size_t WS_OB = WS_OA + (4u << 20);
constexpr size_t WS_KVP = WS_OB + (4u << 20);
constexpr size_t WS_ZP = WS_KVP + (1u << 20);
constexpr size_t WS_END = WS_ZP + (32u << 10);

#define STAMP(k, i)
#define STAMPC(k, i)
#define STAMP_ARG
#define STAMP_PASS

DI unsigned rfl(unsigned x) { return __builtin_amdgcn_readfirstlane(x); }
DI unsigned cvtpk(float lo, float hi) { f32x2 v = {lo, hi}; bf16x2 b = __builtin_convertvector(v, bf16x2); return __builtin_bit_cast(unsigned, b); }
DI bf16x8 pack8(const f32x16& a, int s) {
  u32x4 w = {cvtpk(a[8 * s], a[8 * s + 1]), cvtpk(a[8 * s + 2], a[8 * s + 3]), cvtpk(a[8 * s + 4], a[8 * s + 5]), cvtpk(a[8 * s + 6], a[8 * s + 7])};
  return __builtin_bit_cast(bf16x8, w);
}
DI float rbf(float x) { return (float)(__bf16)x; }
DI float phi(float t) { t = rbf(t); return (t > 0.f ? t + 1.f : __expf(t)) + 1e-6f; }
DI void dma16(__amdgpu_buffer_rsrc_t srd, unsigned voff, unsigned soff, unsigned ldsdst) {
  asm volatile("s_mov_b32 m0, %0\n\ts_nop 0\n\tbuffer_load_dwordx4 %1, %2, %3 offen lds" :: "s"(ldsdst), "v"(voff), "s"(srd), "s"(soff) : "memory");
}
#define WAIT_BAR(N) asm volatile("s_waitcnt vmcnt(" #N ") lgkmcnt(0)\n\ts_barrier" ::: "memory")
DI bf16x8 lds128(const LAS char* p) { return *(const LAS bf16x8*)p; }
DI bf16x8 ldstr(const LAS char* p, int d) {
  s16x4 a = __builtin_amdgcn_ds_read_tr16_b64_v4i16((LAS s16x4*)p), b = __builtin_amdgcn_ds_read_tr16_b64_v4i16((LAS s16x4*)(p + d));
  typedef short s16x8 __attribute__((ext_vector_type(8)));
  s16x8 r = {a[0], a[1], a[2], a[3], b[0], b[1], b[2], b[3]};
  return __builtin_bit_cast(bf16x8, r);
}
DI __amdgpu_buffer_rsrc_t mk_srd(const void* p, unsigned bytes) { return __builtin_amdgcn_make_buffer_rsrc((void*)p, (short)0, (int)bytes, 0x00020000); }

constexpr int G1_X = 32768, G1_W = 24576, G1_WOFF = 2 * G1_X, G1_LDS = 2 * (G1_X + G1_W);
template <bool BR_A>
DI void qkv_body(const u16* __restrict__ x, const u16* __restrict__ w, char* __restrict__ ws, int rb, int cb, LAS char* lds STAMP_ARG) {
  const int tid = threadIdx.x, l = tid & 63, h = l >> 5;
  const int wv = rfl(tid >> 6), wm = wv >> 1, wn = wv & 1;
  const unsigned lds0 = (unsigned)(size_t)lds;
  const __amdgpu_buffer_rsrc_t sx = mk_srd(x, SEQ * DM * 2), sw = mk_srd(w, DM * 3 * DM * 2);
  unsigned vx[2];
#pragma unroll
  for (int i = 0; i < 2; ++i) vx[i] = (unsigned)(rb * 256 + wv * 32 + (l >> 3)) * 2048u + (unsigned)(((l & 7) ^ ((4 * i + (l >> 4)) & 7)) * 16);
  const unsigned vw = (unsigned)(wv * 8 + (l >> 3)) * 6144u + cb * 128 + ((((l >> 2) & 1) ^ ((l >> 4) & 1)) * 64) + (l & 3) * 16;
  const unsigned dx = lds0 + wv * 4096, dw = lds0 + G1_WOFF + wv * 1024;
#define G1_DMA(kt, st, p) do { if ((p) < 4) dma16(sx, vx[(p) & 1], (kt) * 128u + (p) * 16384u, dx + (st) * G1_X + (p) * 1024); \
                               else dma16(sw, vw, (kt) * (64u * 6144u) + ((p) - 4) * 2048u, dw + (st) * G1_W + ((p) - 4) * 8192); } while (0)
  const LAS char* ax[4];
  { const int row = wm * 64 + (l & 31);
#pragma unroll
    for (int ks = 0; ks < 4; ++ks) ax[ks] = lds + row * 128 + (((2 * ks + h) ^ ((row >> 1) & 7)) * 16); }
  const LAS char* aw; { const int q = (l & 15) >> 2; aw = lds + G1_WOFF + (8 * h + q) * 128 + ((wn ^ (q >> 1)) * 64) + ((l >> 4) & 1) * 32 + (l & 3) * 8; }
  f32x16 acc[2][3];
#pragma unroll
  for (int i = 0; i < 2; ++i)
#pragma unroll
    for (int j = 0; j < 3; ++j) acc[i][j] = f32x16{};
  bf16x8 fx[2][2], fw[2][3];
#define G1_RX(b, st, ks, m) fx[b][m] = lds128(ax[ks] + (st) * G1_X + (m) * 4096)
#define G1_RW(b, st, ks, g) fw[b][g] = ldstr(aw + (st) * G1_W + (g) * 8192 + (ks) * 2048, 512)
#define G1_MM(b, m, g) acc[m][g] = ((g) == 2 || (BR_A && (g) == 1)) ? MFMA32(fx[b][m], fw[b][g], acc[m][g]) : MFMA32(fw[b][g], fx[b][m], acc[m][g])
#pragma unroll
  for (int p = 0; p < 7; ++p) G1_DMA(0, 0, p);
#pragma unroll
  for (int p = 0; p < 7; ++p) G1_DMA(1, 1, p);
  WAIT_BAR(7);
  STAMP(0, 1);
  G1_RX(0, 0, 0, 0); G1_RW(0, 0, 0, 0); G1_RX(0, 0, 0, 1); G1_RW(0, 0, 0, 1); G1_RW(0, 0, 0, 2);
#define G1_STEP(b, RD, nst, nks, DM_, dkt, dst, p0) do { SBAR(); \
    G1_MM(b, 0, 0); if (RD) { G1_RX(b ^ 1, nst, nks, 0); G1_RW(b ^ 1, nst, nks, 0); } SBAR(); \
    G1_MM(b, 1, 0); if (RD) { G1_RX(b ^ 1, nst, nks, 1); } if (DM_) G1_DMA(dkt, dst, p0); SBAR(); \
    G1_MM(b, 0, 1); if (RD) { G1_RW(b ^ 1, nst, nks, 1); } if (DM_) G1_DMA(dkt, dst, p0 + 1); SBAR(); \
    G1_MM(b, 1, 1); if (RD) { G1_RW(b ^ 1, nst, nks, 2); } if (DM_) G1_DMA(dkt, dst, p0 + 2); SBAR(); \
    G1_MM(b, 0, 2); if (DM_ && p0 + 3 < 7) G1_DMA(dkt, dst, p0 + 3); SBAR(); \
    G1_MM(b, 1, 2); SBAR(); } while (0)
#define G1_TILE(kt, st, D0, NEXT, D1) do { \
    G1_STEP(0, true, st, 1, D0, (kt) + 1, (st) ^ 1, 4); \
    G1_STEP(1, true, st, 2, false, 0, 0, 0); \
    G1_STEP(0, true, st, 3, false, 0, 0, 0); \
    if (NEXT) WAIT_BAR(0); \
    G1_STEP(1, NEXT, (st) ^ 1, 0, D1, (kt) + 2, st, 0); } while (0)
  G1_TILE(0, 0, false, true, true);
  for (int kt = 1; kt < 13; kt += 2) { G1_TILE(kt, 1, true, true, true); G1_TILE(kt + 1, 0, true, true, true); }
  G1_TILE(13, 1, true, true, true);
  G1_TILE(14, 0, true, true, false);
  G1_TILE(15, 1, false, false, false);
  STAMP(0, 2);
  const int head = 2 * cb + wn, tile0 = rb * 8 + wm * 2;
  if constexpr (!BR_A) {
    constexpr float QS = 0.17677669529663687f * 1.4426950408889634f;
#pragma unroll
    for (int m = 0; m < 2; ++m) {
      char* q = ws + WS_QB + ((size_t)(head * 64 + tile0 + m) * 2048) + l * 16; char* kv = ws + WS_KV + ((size_t)(head * 64 + tile0 + m) * 4096) + l * 16;
      f32x16 t = acc[m][0] * QS;
      *(bf16x8*)q = pack8(t, 0); *(bf16x8*)(q + 1024) = pack8(t, 1);
      *(bf16x8*)kv = pack8(acc[m][1], 0); *(bf16x8*)(kv + 1024) = pack8(acc[m][1], 1);
      *(bf16x8*)(kv + 2048) = pack8(acc[m][2], 0); *(bf16x8*)(kv + 3072) = pack8(acc[m][2], 1);
    }
  } else {
    f32x16 kvp = f32x16{}, zp = f32x16{};
    const u32x4 onesw = {0x3f803f80u, 0x3f803f80u, 0x3f803f80u, 0x3f803f80u}; const bf16x8 ones = __builtin_bit_cast(bf16x8, onesw);
#pragma unroll
    for (int m = 0; m < 2; ++m) {
      char* q = ws + WS_PQ + ((size_t)(head * 64 + tile0 + m) * 2048) + l * 16;
      f32x16 t;
#pragma unroll
      for (int r = 0; r < 16; ++r) t[r] = phi(acc[m][0][r]);
      *(bf16x8*)q = pack8(t, 0); *(bf16x8*)(q + 1024) = pack8(t, 1);
#pragma unroll
      for (int r = 0; r < 16; ++r) t[r] = phi(acc[m][1][r]);
#pragma unroll
      for (int s = 0; s < 2; ++s) { const bf16x8 pk = pack8(t, s); kvp = MFMA32(pk, pack8(acc[m][2], s), kvp); zp = MFMA32(pk, ones, zp); }
    }
    __syncthreads();
    LAS f32x4* red = (LAS f32x4*)lds;
    LAS f32x4* redz = (LAS f32x4*)(lds + 32768);
#pragma unroll
    for (int g = 0; g < 4; ++g) { red[(wv * 4 + g) * 64 + l] = f32x4{kvp[4 * g], kvp[4 * g + 1], kvp[4 * g + 2], kvp[4 * g + 3]};
      if ((l & 31) == 0) redz[(wv * 2 + h) * 4 + g] = f32x4{zp[4 * g], zp[4 * g + 1], zp[4 * g + 2], zp[4 * g + 3]}; }
    __syncthreads();
    f32x4 s = red[((0 + wn) * 4 + wm) * 64 + l] + red[((2 + wn) * 4 + wm) * 64 + l] + red[((4 + wn) * 4 + wm) * 64 + l] + red[((6 + wn) * 4 + wm) * 64 + l];
    *(f32x4*)(ws + WS_KVP + ((size_t)((head * 8 + rb) * 4 + wm) * 64 + l) * 16) = s;
    if (wm == 0 && l < 8) *(f32x4*)(ws + WS_ZP + ((size_t)(head * 8 + rb) * 8 + l) * 16) = redz[(0 + wn) * 8 + l] + redz[(2 + wn) * 8 + l] + redz[(4 + wn) * 8 + l] + redz[(6 + wn) * 8 + l];
  }
#undef G1_DMA
#undef G1_RX
#undef G1_RW
#undef G1_MM
#undef G1_STEP
#undef G1_TILE
}
__global__ __launch_bounds__(512, 2) void qkv_kernel(const u16* xa, const u16* wa, const u16* xb, const u16* wb, char* ws STAMP_ARG) {
  extern __shared__ __attribute__((aligned(16))) char smem[];
  STAMP(0, 0); STAMPC(0, 4);
  const int xcd = blockIdx.x & 7, idx = blockIdx.x >> 3, rb = ((xcd >> 1) & 1) * 4 + (idx & 3), cb = (xcd & 1) * 8 + (idx >> 2);
  if (xcd < 4) qkv_body<true>(xa, wa, ws, rb, cb, (LAS char*)smem); else qkv_body<false>(xb, wb, ws, rb, cb, (LAS char*)smem);
  STAMP(0, 3); STAMPC(0, 5);
}

constexpr int AT_SLOT = 32768, AT_RED = 4 * AT_SLOT, AT_LDS = AT_RED + 8192;
DI float xhalf_max(float v) { auto r = __builtin_amdgcn_permlane32_swap(__float_as_uint(v), __float_as_uint(v), false, false); return fmaxf(__uint_as_float(r[0]), __uint_as_float(r[1])); }
DI float max16(const f32x16& s) { float a = fmaxf(fmaxf(s[0], s[1]), s[2]);
#pragma unroll
  for (int r = 3; r < 15; r += 2) a = fmaxf(fmaxf(a, s[r]), s[r + 1]);
  return fmaxf(a, s[15]); }
__device__ __noinline__ void attn_slow(const char* kv, const char* qp, char* op, int l) {
  const bf16x8 q0 = *(const bf16x8*)(qp + l * 16), q1 = *(const bf16x8*)(qp + 1024 + l * 16);
  f32x16 o = f32x16{}; float m = -INFINITY, sum = 0.f;
  for (int t = 0; t < 64; ++t) {
    const char* p = kv + t * 4096 + l * 16;
    f32x16 s = MFMA32(*(const bf16x8*)p, q0, f32x16{}); s = MFMA32(*(const bf16x8*)(p + 1024), q1, s);
    const float mn = fmaxf(m, xhalf_max(max16(s))), a = __builtin_amdgcn_exp2f(m - mn); m = mn; float ps = 0.f;
#pragma unroll
    for (int r = 0; r < 16; ++r) { s[r] = rbf(__builtin_amdgcn_exp2f(s[r] - mn)); ps += s[r]; o[r] *= a; }
    sum = sum * a + ps;
    o = MFMA32(*(const bf16x8*)(p + 2048), pack8(s, 0), o); o = MFMA32(*(const bf16x8*)(p + 3072), pack8(s, 1), o);
  }
  auto r = __builtin_amdgcn_permlane32_swap(__float_as_uint(sum), __float_as_uint(sum), false, false);
  o *= 1.f / (__uint_as_float(r[0]) + __uint_as_float(r[1]));
  *(bf16x8*)(op + l * 16) = pack8(o, 0); *(bf16x8*)(op + 1024 + l * 16) = pack8(o, 1);
}
__global__ __launch_bounds__(512, 2) void attn_kernel(char* __restrict__ ws STAMP_ARG) {
  extern __shared__ __attribute__((aligned(16))) char smem[];
  LAS char* lds = (LAS char*)smem;
  STAMP(1, 0); STAMPC(1, 4);
  const int tid = threadIdx.x, l = tid & 63, h = l >> 5;
  const int wv = rfl(tid >> 6), kh = wv >> 2, wq = wv & 3;
  const int head = (blockIdx.x & 7) * 4 + ((blockIdx.x >> 3) & 3), qb = blockIdx.x >> 5;
  const unsigned lds0 = (unsigned)(size_t)lds;
  const __amdgpu_buffer_rsrc_t skv = mk_srd(ws + WS_KV + (size_t)head * 262144, 262144);
  const unsigned vkv = l * 16;
#define AT_DMA(j) do { _Pragma("unroll") for (int p = 0; p < 4; ++p) dma16(skv, vkv, (unsigned)((kh * 32 + 4 * (j) + wq) * 4096 + p * 1024), lds0 + ((j) & 3) * AT_SLOT + wv * 4096 + p * 1024); } while (0)
  AT_DMA(0);
  { LAS f32x4* red = (LAS f32x4*)(lds + AT_RED);
    if (tid < 256) { const char* p = ws + WS_KVP + (size_t)head * 32768 + tid * 16; f32x4 s = *(const f32x4*)p;
#pragma unroll
      for (int r = 1; r < 8; ++r) s += *(const f32x4*)(p + r * 4096);
      red[tid] = s; }
    else if (tid < 264) { const char* p = ws + WS_ZP + (size_t)head * 1024 + (tid - 256) * 16; f32x4 s = *(const f32x4*)p;
#pragma unroll
      for (int r = 1; r < 8; ++r) s += *(const f32x4*)(p + r * 128);
      red[tid] = s; }
  }
  const char* pqp = ws + WS_PQ + (size_t)(head * 64 + qb * 8 + wv) * 2048 + l * 16;
  const bf16x8 pq0 = *(const bf16x8*)pqp, pq1 = *(const bf16x8*)(pqp + 1024);
  bf16x8 qf[2][2];
#pragma unroll
  for (int i = 0; i < 2; ++i) { const char* qp = ws + WS_QB + (size_t)(head * 64 + qb * 8 + wq * 2 + i) * 2048 + l * 16; qf[i][0] = *(const bf16x8*)qp; qf[i][1] = *(const bf16x8*)(qp + 1024); }
  asm volatile("" :: "v"(qf[0][0]), "v"(qf[0][1]), "v"(qf[1][0]), "v"(qf[1][1]), "v"(pq0), "v"(pq1));
  WAIT_BAR(0);
  AT_DMA(1); AT_DMA(2); AT_DMA(3);
  STAMP(1, 1);
  { const LAS f32x4* red = (const LAS f32x4*)(lds + AT_RED);
    f32x16 kvf, zf;
#pragma unroll
    for (int g = 0; g < 4; ++g) { const f32x4 a = red[g * 64 + l], b = red[256 + h * 4 + g];
#pragma unroll
      for (int e = 0; e < 4; ++e) { kvf[4 * g + e] = a[e]; zf[4 * g + e] = b[e]; } }
    f32x16 num = MFMA32(pack8(kvf, 0), pq0, f32x16{}); num = MFMA32(pack8(kvf, 1), pq1, num);
    f32x16 den = MFMA32(pack8(zf, 0), pq0, f32x16{}); den = MFMA32(pack8(zf, 1), pq1, den);
    num *= 1.f / (den[0] + 1e-6f);
    char* op = ws + WS_OA + (size_t)((qb * 8 + wv) * 32 + head) * 2048 + l * 16;
    *(bf16x8*)op = pack8(num, 0); *(bf16x8*)(op + 1024) = pack8(num, 1);
  }
  f32x16 negm[2], o[2]; f32x4 ls[2];
  { const bf16x8 k0 = lds128(lds + l * 16), k1 = lds128(lds + 1024 + l * 16);
#pragma unroll
    for (int i = 0; i < 2; ++i) { f32x16 s = MFMA32(k0, qf[i][0], f32x16{}); s = MFMA32(k1, qf[i][1], s); const float m = -xhalf_max(max16(s));
#pragma unroll
      for (int r = 0; r < 16; ++r) negm[i][r] = m;
      o[i] = f32x16{}; ls[i] = f32x4{}; } }
  bf16x8 sel; { const unsigned v = ((l & 3) == 0 && ((l >> 4) & 1) == ((l >> 2) & 1)) ? 0x3f803f80u : 0u; const u32x4 t = {v, v, v, v}; sel = __builtin_bit_cast(bf16x8, t); }
  const LAS char* kvb = lds + kh * 16384 + l * 16;
  for (int j = 0; j < 8; ++j) {
    const LAS char* sp = kvb + (j & 3) * AT_SLOT;
#pragma unroll
    for (int t = 0; t < 4; ++t) {
      const bf16x8 k0 = lds128(sp + t * 4096), k1 = lds128(sp + t * 4096 + 1024), v0 = lds128(sp + t * 4096 + 2048), v1 = lds128(sp + t * 4096 + 3072);
#pragma unroll
      for (int i = 0; i < 2; ++i) {
        f32x16 s = MFMA32(k0, qf[i][0], negm[i]); s = MFMA32(k1, qf[i][1], s);
#pragma unroll
        for (int r = 0; r < 16; ++r) s[r] = __builtin_amdgcn_exp2f(s[r]);
        const bf16x8 p0 = pack8(s, 0), p1 = pack8(s, 1);
        o[i] = MFMA32(v0, p0, o[i]); o[i] = MFMA32(v1, p1, o[i]);
        ls[i] = MFMA16(sel, p0, ls[i]); ls[i] = MFMA16(sel, p1, ls[i]);
      }
    }
    if (j < 4) { WAIT_BAR(8); AT_DMA(j + 4); }
    else if (j == 4) WAIT_BAR(8); else if (j == 5) WAIT_BAR(4); else WAIT_BAR(0);
  }
  STAMP(1, 2);
  { LAS f32x4* ex = (LAS f32x4*)lds;
    const f32x16 og = kh ? o[0] : o[1]; const float lg = kh ? ls[0][0] : ls[1][0];
#pragma unroll
    for (int g = 0; g < 4; ++g) ex[(wv * 5 + g) * 64 + l] = f32x4{og[4 * g], og[4 * g + 1], og[4 * g + 2], og[4 * g + 3]};
    ((LAS float*)&ex[(wv * 5 + 4) * 64])[l] = lg;
    __syncthreads();
    f32x16 of = kh ? o[1] : o[0]; float lf = kh ? ls[1][0] : ls[0][0];
    const int pw = wv ^ 4;
#pragma unroll
    for (int g = 0; g < 4; ++g) { const f32x4 a = ex[(pw * 5 + g) * 64 + l];
#pragma unroll
      for (int e = 0; e < 4; ++e) of[4 * g + e] += a[e]; }
    lf += ((const LAS float*)&ex[(pw * 5 + 4) * 64])[l];
    const int tile = qb * 8 + wq * 2 + kh;
    char* op = ws + WS_OB + (size_t)(tile * 32 + head) * 2048;
    float amax = 0.f;
#pragma unroll
    for (int r = 0; r < 16; ++r) amax = fmaxf(amax, fabsf(of[r]));
    const bool ok = lf < 0x1p64f && amax < 0x1p120f;
    if (__all(ok)) { of *= 1.f / lf; *(bf16x8*)(op + l * 16) = pack8(of, 0); *(bf16x8*)(op + 1024 + l * 16) = pack8(of, 1); }
    else attn_slow(ws + WS_KV + (size_t)head * 262144, ws + WS_QB + (size_t)(head * 64 + tile) * 2048, op, l);
  }
  STAMP(1, 3); STAMPC(1, 5);
#undef AT_DMA
}

constexpr int G3_SLOT = 32768, G3_LDS = 4 * G3_SLOT;
__global__ __launch_bounds__(512, 2) void out_kernel(const u16* woa, const u16* wob, const char* __restrict__ ws, u16* __restrict__ out STAMP_ARG) {
  extern __shared__ __attribute__((aligned(16))) char smem[];
  LAS char* lds = (LAS char*)smem;
  STAMP(2, 0); STAMPC(2, 4);
  const int tid = threadIdx.x, l = tid & 63, h = l >> 5;
  const int wv = rfl(tid >> 6), kg = wv >> 2, wm = (wv >> 1) & 1, wn = wv & 1;
  const int xcd = blockIdx.x & 7, idx = blockIdx.x >> 3, br = xcd >> 2, rb = ((xcd >> 1) & 1) * 8 + (idx & 7), cb = (xcd & 1) * 4 + (idx >> 3);
  const unsigned lds0 = (unsigned)(size_t)lds;
  const __amdgpu_buffer_rsrc_t so = mk_srd(ws + (br ? WS_OB : WS_OA) + (size_t)rb * 262144, 262144), sw = mk_srd(br ? wob : woa, DM * DM * 2);
  const unsigned vo = l * 16 + (wv >> 1) * 65536 + (wv & 1) * 2048;
  const unsigned vw = (unsigned)(wv * 8 + (l >> 3)) * 2048u + cb * 256 + ((((l >> 2) & 1) ^ ((l >> 4) & 1)) * 64) + (l & 3) * 16;
#define G3_DMA(kt, p) do { if ((p) < 2) dma16(so, vo, (kt) * 4096u + (p) * 1024u, lds0 + ((kt) & 3) * G3_SLOT + wv * 2048 + (p) * 1024); \
                           else dma16(sw, vw, (kt) * (64u * 2048u) + ((p) - 2) * 128u, lds0 + ((kt) & 3) * G3_SLOT + 16384 + ((p) - 2) * 8192 + wv * 1024); } while (0)
  const LAS char* ao = lds + wm * 8192 + kg * 2048 + l * 16;
  const LAS char* aw[2];
  { const int q = (l & 15) >> 2, c4 = ((l >> 4) & 1) * 4 + (l & 3), col = 16 * (c4 & 1) + 4 * (c4 >> 1);
#pragma unroll
    for (int n = 0; n < 2; ++n) aw[n] = lds + 16384 + wn * 8192 + (kg * 32 + 4 * h + q) * 128 + ((n ^ (q >> 1)) * 64) + col * 2; }
  f32x16 acc[2][2];
#pragma unroll
  for (int i = 0; i < 2; ++i)
#pragma unroll
    for (int j = 0; j < 2; ++j) acc[i][j] = f32x16{};
#pragma unroll
  for (int kt = 0; kt < 3; ++kt)
#pragma unroll
    for (int p = 0; p < 4; ++p) G3_DMA(kt, p);
  for (int kt = 0; kt < 16; ++kt) {
    if (kt < 14) WAIT_BAR(8); else if (kt == 14) WAIT_BAR(4); else WAIT_BAR(0);
    if (kt == 0) STAMP(2, 1);
    if (kt < 13) {
#pragma unroll
      for (int p = 0; p < 4; ++p) G3_DMA(kt + 3, p); }
    const int so_ = (kt & 3) * G3_SLOT;
#pragma unroll
    for (int s = 0; s < 2; ++s) {
      bf16x8 fo[2], fw[2];
#pragma unroll
      for (int i = 0; i < 2; ++i) { fo[i] = lds128(ao + so_ + i * 4096 + s * 1024); fw[i] = ldstr(aw[i] + so_ + s * 2048, 1024); }
#pragma unroll
      for (int m = 0; m < 2; ++m)
#pragma unroll
        for (int n = 0; n < 2; ++n) acc[m][n] = MFMA32(fw[n], fo[m], acc[m][n]);
    }
  }
  STAMP(2, 2);
  __syncthreads();
  { LAS f32x4* ex = (LAS f32x4*)lds;
#pragma unroll
    for (int n = 0; n < 2; ++n) { const f32x16 og = kg ? acc[0][n] : acc[1][n];
#pragma unroll
      for (int g = 0; g < 4; ++g) ex[(wv * 8 + n * 4 + g) * 64 + l] = f32x4{og[4 * g], og[4 * g + 1], og[4 * g + 2], og[4 * g + 3]}; }
    __syncthreads();
    u16* op = out + (size_t)(rb * 128 + wm * 64 + kg * 32 + (l & 31)) * 2048 + br * 1024 + cb * 128 + wn * 64 + h * 16;
#pragma unroll
    for (int n = 0; n < 2; ++n) { f32x16 of = kg ? acc[1][n] : acc[0][n];
#pragma unroll
      for (int g = 0; g < 4; ++g) { const f32x4 a = ex[((wv ^ 4) * 8 + n * 4 + g) * 64 + l];
#pragma unroll
        for (int e = 0; e < 4; ++e) of[4 * g + e] += a[e]; }
      *(bf16x8*)(op + n * 32) = pack8(of, 0); *(bf16x8*)(op + n * 32 + 8) = pack8(of, 1); }
  }
  STAMP(2, 3); STAMPC(2, 5);
#undef G3_DMA
}

extern "C" void kernel_launch(void* const* d_in, const int* in_sizes, int n_in, void* d_out, int out_size, void* d_ws, size_t ws_size, hipStream_t stream) {
  const u16 *xa = (const u16*)d_in[0], *wa = (const u16*)d_in[1], *woa = (const u16*)d_in[2], *xb = (const u16*)d_in[3], *wb = (const u16*)d_in[4], *wob = (const u16*)d_in[5];
  (void)hipFuncSetAttribute((const void*)qkv_kernel, hipFuncAttributeMaxDynamicSharedMemorySize, G1_LDS);
  (void)hipFuncSetAttribute((const void*)attn_kernel, hipFuncAttributeMaxDynamicSharedMemorySize, AT_LDS);
  (void)hipFuncSetAttribute((const void*)out_kernel, hipFuncAttributeMaxDynamicSharedMemorySize, G3_LDS);
  qkv_kernel<<<256, 512, G1_LDS, stream>>>(xa, wa, xb, wb, (char*)d_ws STAMP_PASS);
  attn_kernel<<<256, 512, AT_LDS, stream>>>((char*)d_ws STAMP_PASS);
  out_kernel<<<256, 512, G3_LDS, stream>>>(woa, wob, (const char*)d_ws, (u16*)d_out STAMP_PASS);
}
```

```cpp
#define STAMPS
#define WT
#define ROT ((rb&3)*4)
#include <hip/hip_runtime.h>
#include <hip/hip_bf16.h>
typedef unsigned short u16;
typedef __bf16 bf16x8 __attribute__((ext_vector_type(8)));
typedef short s16x4 __attribute__((ext_vector_type(4)));
typedef float f32x16 __attribute__((ext_vector_type(16)));
typedef float f32x4 __attribute__((ext_vector_type(4)));
typedef float f32x2 __attribute__((ext_vector_type(2)));
typedef __bf16 bf16x2 __attribute__((ext_vector_type(2)));
typedef unsigned u32x4 __attribute__((ext_vector_type(4)));
typedef unsigned u32x2 __attribute__((ext_vector_type(2)));
#define LAS __attribute__((address_space(3)))
#define DI __device__ __forceinline__
#define SBAR() __builtin_amdgcn_sched_barrier(0)
#define MFMA32(a, b, c) __builtin_amdgcn_mfma_f32_32x32x16_bf16(a, b, c, 0, 0, 0)
#define MFMA16(a, b, c) __builtin_amdgcn_mfma_f32_16x16x32_bf16(a, b, c, 0, 0, 0)

#define PFD 8
#define NOPF 0
#define LATEPF 0
#define NOMM 0
#define NORD 0
constexpr int SEQ = 2048, DM = 1024, NH = 32;
constexpr size_t WS_QB = 0;
constexpr size_t WS_KV = WS_QB + (4u << 20);
constexpr size_t WS_PQ = WS_KV + (8u << 20);
constexpr size_t WS_OA = WS_PQ + (4u << 20);
constexpr size_t WS_OB = WS_OA + (4u << 20);
constexpr size_t WS_KVP = WS_OB + (4u << 20);
constexpr size_t WS_ZP = WS_KVP + (2u << 20);
constexpr size_t WS_END = WS_ZP + (64u << 10);

#define STAMP(k, i) do { if (threadIdx.x == 0) { stamps[((k) * 256 + blockIdx.x) * 16 + (i)] = __builtin_amdgcn_s_memrealtime(); stamps[((k) * 256 + blockIdx.x) * 16 + 8 + (i)] = __builtin_amdgcn_s_memtime(); } } while (0)
#define STAMP_ARG , unsigned long long* stamps
#define STAMP_PASS , (unsigned long long*)((char*)d_ws + (64u << 20))

DI unsigned rfl(unsigned x) { return __builtin_amdgcn_readfirstlane(x); }
DI unsigned cvtpk(float lo, float hi) { f32x2 v = {lo, hi}; bf16x2 b = __builtin_convertvector(v, bf16x2); return __builtin_bit_cast(unsigned, b); }
DI bf16x8 pack8(const f32x16& a, int s) {
  u32x4 w = {cvtpk(a[8 * s], a[8 * s + 1]), cvtpk(a[8 * s + 2], a[8 * s + 3]), cvtpk(a[8 * s + 4], a[8 * s + 5]), cvtpk(a[8 * s + 6], a[8 * s + 7])};
  return __builtin_bit_cast(bf16x8, w);
}
DI float rbf(float x) { return (float)(__bf16)x; }
DI float phi(float t) { float e; const float u = t * 1.4426950408889634f; asm("v_exp_f32 %0, %1 clamp" : "=v"(e) : "v"(u)); return (fmaxf(t, 0.f) + 1e-6f) + e; }
DI void dma16(__amdgpu_buffer_rsrc_t srd, unsigned voff, unsigned soff, unsigned ldsdst) {
  asm volatile("s_mov_b32 m0, %0\n\ts_nop 0\n\tbuffer_load_dwordx4 %1, %2, %3 offen lds" :: "s"(ldsdst), "v"(voff), "s"(srd), "s"(soff) : "memory");
}
DI void warm4(__amdgpu_buffer_rsrc_t srd, unsigned voff, unsigned soff, unsigned ldsdst) {
  asm volatile("s_mov_b32 m0, %0\n\ts_nop 0\n\tbuffer_load_dword %1, %2, %3 offen lds" :: "s"(ldsdst), "v"(voff), "s"(srd), "s"(soff) : "memory");
}
#define WAIT_BAR(N) asm volatile("s_waitcnt vmcnt(" #N ") lgkmcnt(0)\n\ts_barrier" ::: "memory")
#define WAIT_LBAR() asm volatile("s_waitcnt lgkmcnt(0)\n\ts_barrier" ::: "memory")
DI void ST16(char* p, bf16x8 v) { asm volatile("global_store_dwordx4 %0, %1, off sc1\n\ts_nop 1" :: "v"(p), "v"(v) : "memory"); }
DI bf16x8 lds128(const LAS char* p) { return *(const LAS bf16x8*)p; }
DI bf16x8 ldstr(const LAS char* p, int d) {
  s16x4 a = __builtin_amdgcn_ds_read_tr16_b64_v4i16((LAS s16x4*)p), b = __builtin_amdgcn_ds_read_tr16_b64_v4i16((LAS s16x4*)(p + d));
  typedef short s16x8 __attribute__((ext_vector_type(8)));
  s16x8 r = {a[0], a[1], a[2], a[3], b[0], b[1], b[2], b[3]};
  return __builtin_bit_cast(bf16x8, r);
}
DI __amdgpu_buffer_rsrc_t mk_srd(const void* p, unsigned bytes) { return __builtin_amdgcn_make_buffer_rsrc((void*)p, (short)0, (int)bytes, 0x00020000); }

constexpr int G1_X = 16384, G1_SLOT = 40960, G1_NS = 4, G1_LDS = G1_NS * G1_SLOT;
#define G1_RX(b, so_, ks, m) fx[b][m] = lds128(ax[ks] + (so_) + (m) * 4096)
#define G1_RW(b, so_, ks, g) fw[b][g] = ldstr(aw + (so_) + (g) * 8192 + (ks) * 2048, 512)
#define G1_MM(A_, Z, b, m, g) acc[m][g] = ((g) == 2 || ((A_) && (g) == 1)) ? MFMA32(fx[b][m], fw[b][g], (Z) ? f32x16{} : acc[m][g]) : MFMA32(fw[b][g], fx[b][m], (Z) ? f32x16{} : acc[m][g])
#define G1_STEP(A_, Z, b, RD, so_, nks, X0, X1, X2) do { SBAR(); \
    G1_MM(A_, Z, b, 0, 0); if (RD) { G1_RX(b ^ 1, so_, nks, 0); G1_RW(b ^ 1, so_, nks, 0); } SBAR(); \
    G1_MM(A_, Z, b, 1, 0); if (RD) { G1_RX(b ^ 1, so_, nks, 1); } SBAR(); \
    G1_MM(A_, Z, b, 0, 1); if (RD) { G1_RW(b ^ 1, so_, nks, 1); } SBAR(); \
    G1_MM(A_, Z, b, 1, 1); if (RD) { G1_RW(b ^ 1, so_, nks, 2); } X0; SBAR(); \
    G1_MM(A_, Z, b, 0, 2); X1; SBAR(); \
    G1_MM(A_, Z, b, 1, 2); X2; SBAR(); } while (0)
#define G1_NEXT(so) ((so) == (G1_NS - 1) * G1_SLOT ? 0 : (so) + G1_SLOT)
#define G1_TILE(A_) do { const int sn = G1_NEXT(so); \
    G1_STEP(A_, false, 0, true, so, 1, , , ); G1_STEP(A_, false, 1, true, so, 2, , , ); G1_STEP(A_, false, 0, true, so, 3, , , ); \
    WAIT_LBAR(); G1_STEP(A_, false, 1, true, sn, 0, , , ); so = sn; } while (0)
__global__ __launch_bounds__(512, 2) void qkv_kernel(const u16* xa, const u16* wa, const u16* xb, const u16* wb, char* __restrict__ ws STAMP_ARG) {
  extern __shared__ __attribute__((aligned(16))) char smem[];
  LAS char* lds = (LAS char*)smem;
  STAMP(0, 0);
  const int tid = threadIdx.x, l = tid & 63, h = l >> 5;
  const int wv = rfl(tid >> 6), w4 = wv & 3, wm = w4 >> 1, wn = w4 & 1;
  const int xcd = blockIdx.x & 7, idx = blockIdx.x >> 3, rb = (xcd & 1) * 8 + (idx & 7), cb = (xcd >> 1) * 4 + (idx >> 3);
  const unsigned lds0 = (unsigned)(size_t)lds;
  if (wv >= 4) {
    const __amdgpu_buffer_rsrc_t sxa = mk_srd(xa, SEQ * DM * 2), swa = mk_srd(wa, DM * 3 * DM * 2), sxb = mk_srd(xb, SEQ * DM * 2), swb = mk_srd(wb, DM * 3 * DM * 2);
    unsigned vx[2];
#pragma unroll
    for (int i = 0; i < 2; ++i) vx[i] = (unsigned)(rb * 128 + w4 * 32 + (l >> 3)) * 2048u + (unsigned)(((l & 7) ^ ((4 * i + (l >> 4)) & 7)) * 16);
    const unsigned vw = (unsigned)(w4 * 16 + (l >> 3)) * 6144u + cb * 128 + ((((l >> 2) & 1) ^ ((l >> 4) & 1)) * 64) + (l & 3) * 16;
    unsigned sl = 0;
#define G1_DMA(g) do { const __amdgpu_buffer_rsrc_t sx = (g) < 16 ? sxb : sxa, sw = (g) < 16 ? swb : swa; const unsigned kt = ((g) + ROT) & 15; \
    _Pragma("unroll") for (int p = 0; p < 4; ++p) { dma16(sx, vx[p & 1], kt * 128u + p * 16384u, lds0 + sl + w4 * 4096 + p * 1024); \
      if (p < 3) { dma16(sw, vw, kt * (64u * 6144u) + p * 2048u, lds0 + sl + G1_X + p * 8192 + w4 * 2048); dma16(sw, vw, kt * (64u * 6144u) + 8u * 6144u + p * 2048u, lds0 + sl + G1_X + p * 8192 + w4 * 2048 + 1024); } } \
    sl = G1_NEXT(sl); } while (0)
    G1_DMA(0); G1_DMA(1); G1_DMA(2); G1_DMA(3);
    WAIT_BAR(30);
    for (int g = 0; g < 28; ++g) { WAIT_BAR(20); G1_DMA(g + 4); }
    WAIT_BAR(20); WAIT_BAR(10); WAIT_BAR(0);
#undef G1_DMA
    __syncthreads();
  } else {
    const LAS char* ax[4];
    { const int row = wm * 64 + (l & 31);
#pragma unroll
      for (int ks = 0; ks < 4; ++ks) ax[ks] = lds + row * 128 + (((2 * ks + h) ^ ((row >> 1) & 7)) * 16); }
    const LAS char* aw; { const int q = (l & 15) >> 2; aw = lds + G1_X + (8 * h + q) * 128 + ((wn ^ (q >> 1)) * 64) + ((l >> 4) & 1) * 32 + (l & 3) * 8; }
    f32x16 acc[2][3]; bf16x8 fx[2][2], fw[2][3]; int so = 0;
    const int head = 2 * cb + wn, tile0 = rb * 4 + wm * 2;
    WAIT_LBAR();
    STAMP(0, 1);
    G1_RX(0, 0, 0, 0); G1_RW(0, 0, 0, 0); G1_RX(0, 0, 0, 1); G1_RW(0, 0, 0, 1); G1_RW(0, 0, 0, 2);
    G1_STEP(false, true, 0, true, 0, 1, , , ); G1_STEP(false, false, 1, true, 0, 2, , , ); G1_STEP(false, false, 0, true, 0, 3, , , );
    WAIT_LBAR(); G1_STEP(false, false, 1, true, G1_SLOT, 0, , , ); so = G1_SLOT;
    for (int kt = 1; kt < 16; ++kt) G1_TILE(false);
    STAMP(0, 2);
    bf16x8 pk[2][3][2];
    { constexpr float QS = 0.17677669529663687f * 1.4426950408889634f;
#pragma unroll
      for (int m = 0; m < 2; ++m) { acc[m][0] *= QS;
#pragma unroll
        for (int g = 0; g < 3; ++g) { pk[m][g][0] = pack8(acc[m][g], 0); pk[m][g][1] = pack8(acc[m][g], 1); } } }
    char* qp = ws + WS_QB + ((size_t)(head * 64 + tile0) * 2048) + l * 16; char* kvp_ = ws + WS_KV + ((size_t)(head * 64 + tile0) * 4096) + l * 16;
    STAMP(0, 3);
    { const int sn = G1_NEXT(so);
      G1_STEP(true, true, 0, true, so, 1, ST16(kvp_, pk[0][1][0]), ST16(kvp_ + 1024, pk[0][1][1]), ST16(kvp_ + 2048, pk[0][2][0]));
      G1_STEP(true, false, 1, true, so, 2, ST16(kvp_ + 3072, pk[0][2][1]), ST16(kvp_ + 4096, pk[1][1][0]), ST16(kvp_ + 5120, pk[1][1][1]));
      G1_STEP(true, false, 0, true, so, 3, ST16(kvp_ + 6144, pk[1][2][0]), ST16(kvp_ + 7168, pk[1][2][1]), ST16(qp, pk[0][0][0]));
      WAIT_LBAR();
      G1_STEP(true, false, 1, true, sn, 0, ST16(qp + 1024, pk[0][0][1]), ST16(qp + 2048, pk[1][0][0]), ST16(qp + 3072, pk[1][0][1])); so = sn; }
    for (int kt = 1; kt < 15; ++kt) G1_TILE(true);
    G1_STEP(true, false, 0, true, so, 1, , , ); G1_STEP(true, false, 1, true, so, 2, , , ); G1_STEP(true, false, 0, true, so, 3, , , ); G1_STEP(true, false, 1, false, 0, 0, , , );
    STAMP(0, 4);
    f32x16 kvp = f32x16{}, zp = f32x16{};
    const u32x4 onesw = {0x3f803f80u, 0x3f803f80u, 0x3f803f80u, 0x3f803f80u}; const bf16x8 ones = __builtin_bit_cast(bf16x8, onesw);
#pragma unroll
    for (int m = 0; m < 2; ++m) {
      char* q = ws + WS_PQ + ((size_t)(head * 64 + tile0 + m) * 2048) + l * 16;
      ST16(q, pack8(acc[m][0], 0)); ST16(q + 1024, pack8(acc[m][0], 1));
      f32x16 t;
#pragma unroll
      for (int r = 0; r < 16; ++r) t[r] = phi(acc[m][1][r]);
#pragma unroll
      for (int s = 0; s < 2; ++s) { const bf16x8 pf = pack8(t, s); kvp = MFMA32(pf, pack8(acc[m][2], s), kvp); zp = MFMA32(pf, ones, zp); }
    }
    LAS f32x4* red = (LAS f32x4*)lds;
#pragma unroll
    for (int g = 0; g < 2; ++g) { const int o = wm ? 4 * g : 8 + 4 * g; red[(wv * 2 + g) * 64 + l] = f32x4{kvp[o], kvp[o + 1], kvp[o + 2], kvp[o + 3]}; }
    if ((l & 31) == 0) {
#pragma unroll
      for (int g = 0; g < 4; ++g) red[512 + (wv * 2 + h) * 4 + g] = f32x4{zp[4 * g], zp[4 * g + 1], zp[4 * g + 2], zp[4 * g + 3]}; }
    __syncthreads();
#pragma unroll
    for (int g = 0; g < 2; ++g) { const int o = wm ? 8 + 4 * g : 4 * g;
      *(f32x4*)(ws + WS_KVP + ((size_t)((head * 16 + rb) * 4 + 2 * wm + g) * 64 + l) * 16) = red[((wv ^ 2) * 2 + g) * 64 + l] + f32x4{kvp[o], kvp[o + 1], kvp[o + 2], kvp[o + 3]}; }
    if (wm == 0 && l < 8) *(f32x4*)(ws + WS_ZP + ((size_t)(head * 16 + rb) * 8 + l) * 16) = red[512 + wv * 8 + l] + red[512 + (wv + 2) * 8 + l];
  }
  STAMP(0, 5);
}
#undef G1_RX
#undef G1_RW
#undef G1_MM
#undef G1_STEP
#undef G1_TILE

#define AT_LOOP_ASM \
  "; prologue: K(0), V0(0); P = V1 = 0; scores of unit a(0)\n\t" \
  "ds_read_b128 v[144:147], v181\n\t" \
  "ds_read_b128 v[148:151], v181 offset:1024\n\t" \
  "ds_read_b128 v[152:155], v181 offset:2048\n\t" \
  "v_mov_b32 v188, 0\n\t" \
  "v_mov_b32 v189, 0\n\t" \
  "v_mov_b32 v190, 0\n\t" \
  "v_mov_b32 v191, 0\n\t" \
  "v_mov_b32 v192, 0\n\t" \
  "v_mov_b32 v193, 0\n\t" \
  "v_mov_b32 v194, 0\n\t" \
  "v_mov_b32 v195, 0\n\t" \
  "v_mov_b32 v156, 0\n\t" \
  "v_mov_b32 v157, 0\n\t" \
  "v_mov_b32 v158, 0\n\t" \
  "v_mov_b32 v159, 0\n\t" \
  "s_mov_b32 s53, 0x07060302\n\t" \
  "s_waitcnt lgkmcnt(1)\n\t" \
  "v_mfma_f32_32x32x16_bf16 v[32:47], v[144:147], v[128:131], v[96:111]\n\t" \
  "v_mfma_f32_32x32x16_bf16 v[32:47], v[148:151], v[132:135], v[32:47]\n\t" \
  "s_nop 11\n\t" \
  "s_mov_b32 s52, 0\n\t" \
  "1:\n\t" \
  "v_mfma_f32_32x32x16_bf16 v[48:63], v[144:147], v[136:139], v[112:127]\n\t" \
  "ds_read_b128 v[144:147], v181 offset:4096\n\t" \
  "v_exp_f32 v32, v32\n\t" \
  "v_exp_f32 v33, v33\n\t" \
  "v_exp_f32 v34, v34\n\t" \
  "v_cvt_pk_bf16_f32 v160, v32, v33\n\t" \
  "v_mfma_f32_32x32x16_bf16 v[48:63], v[148:151], v[140:143], v[48:63]\n\t" \
  "ds_read_b128 v[148:151], v181 offset:5120\n\t" \
  "v_exp_f32 v35, v35\n\t" \
  "v_exp_f32 v36, v36\n\t" \
  "v_exp_f32 v37, v37\n\t" \
  "v_cvt_pk_bf16_f32 v161, v34, v35\n\t" \
  "v_smfmac_f32_16x16x64_bf16 v[172:175], v[176:179], v[188:195], v180\n\t" \
  "v_exp_f32 v38, v38\n\t" \
  "v_exp_f32 v39, v39\n\t" \
  "v_exp_f32 v40, v40\n\t" \
  "v_cvt_pk_bf16_f32 v162, v36, v37\n\t" \
  "v_mfma_f32_32x32x16_bf16 v[80:95], v[156:159], v[192:195], v[80:95]\n\t" \
  "ds_read_b128 v[156:159], v181 offset:3072\n\t" \
  "v_exp_f32 v41, v41\n\t" \
  "v_exp_f32 v42, v42\n\t" \
  "v_cvt_pk_bf16_f32 v163, v38, v39\n\t" \
  "v_exp_f32 v43, v43\n\t" \
  "v_exp_f32 v44, v44\n\t" \
  "v_exp_f32 v45, v45\n\t" \
  "s_waitcnt lgkmcnt(3)\n\t" \
  "v_mfma_f32_32x32x16_bf16 v[64:79], v[152:155], v[160:163], v[64:79]\n\t" \
  "v_exp_f32 v46, v46\n\t" \
  "v_exp_f32 v47, v47\n\t" \
  "v_cvt_pk_bf16_f32 v164, v40, v41\n\t" \
  "v_cvt_pk_bf16_f32 v165, v42, v43\n\t" \
  "v_cvt_pk_bf16_f32 v166, v44, v45\n\t" \
  "v_cvt_pk_bf16_f32 v167, v46, v47\n\t" \
  "s_waitcnt lgkmcnt(1)\n\t" \
  "v_mfma_f32_32x32x16_bf16 v[32:47], v[144:147], v[128:131], v[96:111]\n\t" \
  "v_exp_f32 v48, v48\n\t" \
  "v_exp_f32 v49, v49\n\t" \
  "v_exp_f32 v50, v50\n\t" \
  "v_cvt_pk_bf16_f32 v188, v48, v49\n\t" \
  "v_mfma_f32_32x32x16_bf16 v[32:47], v[148:151], v[132:135], v[32:47]\n\t" \
  "v_exp_f32 v51, v51\n\t" \
  "v_exp_f32 v52, v52\n\t" \
  "v_exp_f32 v53, v53\n\t" \
  "v_cvt_pk_bf16_f32 v189, v50, v51\n\t" \
  "v_smfmac_f32_16x16x64_bf16 v[168:171], v[176:179], v[160:167], v180\n\t" \
  "v_exp_f32 v54, v54\n\t" \
  "v_exp_f32 v55, v55\n\t" \
  "v_exp_f32 v56, v56\n\t" \
  "v_cvt_pk_bf16_f32 v190, v52, v53\n\t" \
  "s_waitcnt lgkmcnt(0)\n\t" \
  "v_mfma_f32_32x32x16_bf16 v[64:79], v[156:159], v[164:167], v[64:79]\n\t" \
  "v_exp_f32 v57, v57\n\t" \
  "v_exp_f32 v58, v58\n\t" \
  "v_cvt_pk_bf16_f32 v191, v54, v55\n\t" \
  "v_exp_f32 v59, v59\n\t" \
  "v_exp_f32 v60, v60\n\t" \
  "v_exp_f32 v61, v61\n\t" \
  "v_mfma_f32_32x32x16_bf16 v[80:95], v[152:155], v[188:191], v[80:95]\n\t" \
  "ds_read_b128 v[152:155], v181 offset:6144\n\t" \
  "v_exp_f32 v62, v62\n\t" \
  "v_exp_f32 v63, v63\n\t" \
  "v_cvt_pk_bf16_f32 v192, v56, v57\n\t" \
  "v_cvt_pk_bf16_f32 v193, v58, v59\n\t" \
  "v_cvt_pk_bf16_f32 v194, v60, v61\n\t" \
  "v_cvt_pk_bf16_f32 v195, v62, v63\n\t" \
  "v_mfma_f32_32x32x16_bf16 v[48:63], v[144:147], v[136:139], v[112:127]\n\t" \
  "ds_read_b128 v[144:147], v181 offset:8192\n\t" \
  "v_exp_f32 v32, v32\n\t" \
  "v_exp_f32 v33, v33\n\t" \
  "v_exp_f32 v34, v34\n\t" \
  "v_cvt_pk_bf16_f32 v160, v32, v33\n\t" \
  "v_mfma_f32_32x32x16_bf16 v[48:63], v[148:151], v[140:143], v[48:63]\n\t" \
  "ds_read_b128 v[148:151], v181 offset:9216\n\t" \
  "v_exp_f32 v35, v35\n\t" \
  "v_exp_f32 v36, v36\n\t" \
  "v_exp_f32 v37, v37\n\t" \
  "v_cvt_pk_bf16_f32 v161, v34, v35\n\t" \
  "v_smfmac_f32_16x16x64_bf16 v[172:175], v[176:179], v[188:195], v180\n\t" \
  "v_exp_f32 v38, v38\n\t" \
  "v_exp_f32 v39, v39\n\t" \
  "v_exp_f32 v40, v40\n\t" \
  "v_cvt_pk_bf16_f32 v162, v36, v37\n\t" \
  "v_mfma_f32_32x32x16_bf16 v[80:95], v[156:159], v[192:195], v[80:95]\n\t" \
  "ds_read_b128 v[156:159], v181 offset:7168\n\t" \
  "v_exp_f32 v41, v41\n\t" \
  "v_exp_f32 v42, v42\n\t" \
  "v_cvt_pk_bf16_f32 v163, v38, v39\n\t" \
  "v_exp_f32 v43, v43\n\t" \
  "v_exp_f32 v44, v44\n\t" \
  "v_exp_f32 v45, v45\n\t" \
  "s_waitcnt lgkmcnt(3)\n\t" \
  "v_mfma_f32_32x32x16_bf16 v[64:79], v[152:155], v[160:163], v[64:79]\n\t" \
  "v_exp_f32 v46, v46\n\t" \
  "v_exp_f32 v47, v47\n\t" \
  "v_cvt_pk_bf16_f32 v164, v40, v41\n\t" \
  "v_cvt_pk_bf16_f32 v165, v42, v43\n\t" \
  "v_cvt_pk_bf16_f32 v166, v44, v45\n\t" \
  "v_cvt_pk_bf16_f32 v167, v46, v47\n\t" \
  "s_waitcnt lgkmcnt(1)\n\t" \
  "v_mfma_f32_32x32x16_bf16 v[32:47], v[144:147], v[128:131], v[96:111]\n\t" \
  "v_exp_f32 v48, v48\n\t" \
  "v_exp_f32 v49, v49\n\t" \
  "v_exp_f32 v50, v50\n\t" \
  "v_cvt_pk_bf16_f32 v188, v48, v49\n\t" \
  "v_mfma_f32_32x32x16_bf16 v[32:47], v[148:151], v[132:135], v[32:47]\n\t" \
  "v_exp_f32 v51, v51\n\t" \
  "v_exp_f32 v52, v52\n\t" \
  "v_exp_f32 v53, v53\n\t" \
  "v_cvt_pk_bf16_f32 v189, v50, v51\n\t" \
  "v_smfmac_f32_16x16x64_bf16 v[168:171], v[176:179], v[160:167], v180\n\t" \
  "v_exp_f32 v54, v54\n\t" \
  "v_exp_f32 v55, v55\n\t" \
  "v_exp_f32 v56, v56\n\t" \
  "v_cvt_pk_bf16_f32 v190, v52, v53\n\t" \
  "s_waitcnt lgkmcnt(0)\n\t" \
  "v_mfma_f32_32x32x16_bf16 v[64:79], v[156:159], v[164:167], v[64:79]\n\t" \
  "v_exp_f32 v57, v57\n\t" \
  "v_exp_f32 v58, v58\n\t" \
  "v_cvt_pk_bf16_f32 v191, v54, v55\n\t" \
  "v_exp_f32 v59, v59\n\t" \
  "v_exp_f32 v60, v60\n\t" \
  "v_exp_f32 v61, v61\n\t" \
  "v_mfma_f32_32x32x16_bf16 v[80:95], v[152:155], v[188:191], v[80:95]\n\t" \
  "ds_read_b128 v[152:155], v181 offset:10240\n\t" \
  "v_exp_f32 v62, v62\n\t" \
  "v_exp_f32 v63, v63\n\t" \
  "v_cvt_pk_bf16_f32 v192, v56, v57\n\t" \
  "v_cvt_pk_bf16_f32 v193, v58, v59\n\t" \
  "v_cvt_pk_bf16_f32 v194, v60, v61\n\t" \
  "v_cvt_pk_bf16_f32 v195, v62, v63\n\t" \
  "v_mfma_f32_32x32x16_bf16 v[48:63], v[144:147], v[136:139], v[112:127]\n\t" \
  "ds_read_b128 v[144:147], v181 offset:12288\n\t" \
  "v_exp_f32 v32, v32\n\t" \
  "v_exp_f32 v33, v33\n\t" \
  "v_exp_f32 v34, v34\n\t" \
  "v_cvt_pk_bf16_f32 v160, v32, v33\n\t" \
  "v_mfma_f32_32x32x16_bf16 v[48:63], v[148:151], v[140:143], v[48:63]\n\t" \
  "ds_read_b128 v[148:151], v181 offset:13312\n\t" \
  "v_exp_f32 v35, v35\n\t" \
  "v_exp_f32 v36, v36\n\t" \
  "v_exp_f32 v37, v37\n\t" \
  "v_cvt_pk_bf16_f32 v161, v34, v35\n\t" \
  "v_smfmac_f32_16x16x64_bf16 v[172:175], v[176:179], v[188:195], v180\n\t" \
  "v_exp_f32 v38, v38\n\t" \
  "v_exp_f32 v39, v39\n\t" \
  "v_exp_f32 v40, v40\n\t" \
  "v_cvt_pk_bf16_f32 v162, v36, v37\n\t" \
  "v_mfma_f32_32x32x16_bf16 v[80:95], v[156:159], v[192:195], v[80:95]\n\t" \
  "ds_read_b128 v[156:159], v181 offset:11264\n\t" \
  "v_exp_f32 v41, v41\n\t" \
  "v_exp_f32 v42, v42\n\t" \
  "v_cvt_pk_bf16_f32 v163, v38, v39\n\t" \
  "v_exp_f32 v43, v43\n\t" \
  "v_exp_f32 v44, v44\n\t" \
  "v_exp_f32 v45, v45\n\t" \
  "s_waitcnt lgkmcnt(3)\n\t" \
  "v_mfma_f32_32x32x16_bf16 v[64:79], v[152:155], v[160:163], v[64:79]\n\t" \
  "v_exp_f32 v46, v46\n\t" \
  "v_exp_f32 v47, v47\n\t" \
  "v_cvt_pk_bf16_f32 v164, v40, v41\n\t" \
  "v_cvt_pk_bf16_f32 v165, v42, v43\n\t" \
  "v_cvt_pk_bf16_f32 v166, v44, v45\n\t" \
  "v_cvt_pk_bf16_f32 v167, v46, v47\n\t" \
  "s_waitcnt lgkmcnt(1)\n\t" \
  "v_mfma_f32_32x32x16_bf16 v[32:47], v[144:147], v[128:131], v[96:111]\n\t" \
  "v_exp_f32 v48, v48\n\t" \
  "v_exp_f32 v49, v49\n\t" \
  "v_exp_f32 v50, v50\n\t" \
  "v_cvt_pk_bf16_f32 v188, v48, v49\n\t" \
  "v_mfma_f32_32x32x16_bf16 v[32:47], v[148:151], v[132:135], v[32:47]\n\t" \
  "v_exp_f32 v51, v51\n\t" \
  "v_exp_f32 v52, v52\n\t" \
  "v_exp_f32 v53, v53\n\t" \
  "v_cvt_pk_bf16_f32 v189, v50, v51\n\t" \
  "v_smfmac_f32_16x16x64_bf16 v[168:171], v[176:179], v[160:167], v180\n\t" \
  "v_exp_f32 v54, v54\n\t" \
  "v_exp_f32 v55, v55\n\t" \
  "v_exp_f32 v56, v56\n\t" \
  "v_cvt_pk_bf16_f32 v190, v52, v53\n\t" \
  "s_waitcnt lgkmcnt(0)\n\t" \
  "v_mfma_f32_32x32x16_bf16 v[64:79], v[156:159], v[164:167], v[64:79]\n\t" \
  "v_exp_f32 v57, v57\n\t" \
  "v_exp_f32 v58, v58\n\t" \
  "v_cvt_pk_bf16_f32 v191, v54, v55\n\t" \
  "v_exp_f32 v59, v59\n\t" \
  "v_exp_f32 v60, v60\n\t" \
  "v_exp_f32 v61, v61\n\t" \
  "v_mfma_f32_32x32x16_bf16 v[80:95], v[152:155], v[188:191], v[80:95]\n\t" \
  "ds_read_b128 v[152:155], v181 offset:14336\n\t" \
  "v_exp_f32 v62, v62\n\t" \
  "v_exp_f32 v63, v63\n\t" \
  "v_cvt_pk_bf16_f32 v192, v56, v57\n\t" \
  "v_cvt_pk_bf16_f32 v193, v58, v59\n\t" \
  "v_cvt_pk_bf16_f32 v194, v60, v61\n\t" \
  "v_cvt_pk_bf16_f32 v195, v62, v63\n\t" \
  "s_waitcnt vmcnt(4)\n\t" \
  "s_barrier\n\t" \
  "v_mfma_f32_32x32x16_bf16 v[48:63], v[144:147], v[136:139], v[112:127]\n\t" \
  "ds_read_b128 v[144:147], v182 offset:0\n\t" \
  "s_mov_b32 m0, s50\n\t" \
  "v_exp_f32 v32, v32\n\t" \
  "v_exp_f32 v33, v33\n\t" \
  "v_exp_f32 v34, v34\n\t" \
  "v_cvt_pk_bf16_f32 v160, v32, v33\n\t" \
  "v_mfma_f32_32x32x16_bf16 v[48:63], v[148:151], v[140:143], v[48:63]\n\t" \
  "ds_read_b128 v[148:151], v182 offset:1024\n\t" \
  "buffer_load_dwordx4 v183, s[44:47], s51 offen offset:0 lds\n\t" \
  "v_exp_f32 v35, v35\n\t" \
  "v_exp_f32 v36, v36\n\t" \
  "v_exp_f32 v37, v37\n\t" \
  "v_cvt_pk_bf16_f32 v161, v34, v35\n\t" \
  "v_smfmac_f32_16x16x64_bf16 v[172:175], v[176:179], v[188:195], v180\n\t" \
  "buffer_load_dwordx4 v183, s[44:47], s51 offen offset:1024 lds\n\t" \
  "v_exp_f32 v38, v38\n\t" \
  "v_exp_f32 v39, v39\n\t" \
  "v_exp_f32 v40, v40\n\t" \
  "v_cvt_pk_bf16_f32 v162, v36, v37\n\t" \
  "v_mfma_f32_32x32x16_bf16 v[80:95], v[156:159], v[192:195], v[80:95]\n\t" \
  "ds_read_b128 v[156:159], v181 offset:15360\n\t" \
  "buffer_load_dwordx4 v183, s[44:47], s51 offen offset:2048 lds\n\t" \
  "v_exp_f32 v41, v41\n\t" \
  "v_exp_f32 v42, v42\n\t" \
  "v_cvt_pk_bf16_f32 v163, v38, v39\n\t" \
  "v_exp_f32 v43, v43\n\t" \
  "v_exp_f32 v44, v44\n\t" \
  "v_exp_f32 v45, v45\n\t" \
  "s_waitcnt lgkmcnt(3)\n\t" \
  "v_mfma_f32_32x32x16_bf16 v[64:79], v[152:155], v[160:163], v[64:79]\n\t" \
  "buffer_load_dwordx4 v183, s[44:47], s51 offen offset:3072 lds\n\t" \
  "v_exp_f32 v46, v46\n\t" \
  "v_exp_f32 v47, v47\n\t" \
  "v_cvt_pk_bf16_f32 v164, v40, v41\n\t" \
  "v_cvt_pk_bf16_f32 v165, v42, v43\n\t" \
  "v_cvt_pk_bf16_f32 v166, v44, v45\n\t" \
  "v_cvt_pk_bf16_f32 v167, v46, v47\n\t" \
  "s_waitcnt lgkmcnt(1)\n\t" \
  "v_mfma_f32_32x32x16_bf16 v[32:47], v[144:147], v[128:131], v[96:111]\n\t" \
  "v_exp_f32 v48, v48\n\t" \
  "v_exp_f32 v49, v49\n\t" \
  "v_exp_f32 v50, v50\n\t" \
  "v_cvt_pk_bf16_f32 v188, v48, v49\n\t" \
  "v_mfma_f32_32x32x16_bf16 v[32:47], v[148:151], v[132:135], v[32:47]\n\t" \
  "v_exp_f32 v51, v51\n\t" \
  "v_exp_f32 v52, v52\n\t" \
  "v_exp_f32 v53, v53\n\t" \
  "v_cvt_pk_bf16_f32 v189, v50, v51\n\t" \
  "v_smfmac_f32_16x16x64_bf16 v[168:171], v[176:179], v[160:167], v180\n\t" \
  "v_exp_f32 v54, v54\n\t" \
  "v_exp_f32 v55, v55\n\t" \
  "v_exp_f32 v56, v56\n\t" \
  "v_cvt_pk_bf16_f32 v190, v52, v53\n\t" \
  "s_waitcnt lgkmcnt(0)\n\t" \
  "v_mfma_f32_32x32x16_bf16 v[64:79], v[156:159], v[164:167], v[64:79]\n\t" \
  "v_exp_f32 v57, v57\n\t" \
  "v_exp_f32 v58, v58\n\t" \
  "v_cvt_pk_bf16_f32 v191, v54, v55\n\t" \
  "v_exp_f32 v59, v59\n\t" \
  "v_exp_f32 v60, v60\n\t" \
  "v_exp_f32 v61, v61\n\t" \
  "v_mfma_f32_32x32x16_bf16 v[80:95], v[152:155], v[188:191], v[80:95]\n\t" \
  "ds_read_b128 v[152:155], v182 offset:2048\n\t" \
  "v_exp_f32 v62, v62\n\t" \
  "v_exp_f32 v63, v63\n\t" \
  "v_cvt_pk_bf16_f32 v192, v56, v57\n\t" \
  "v_cvt_pk_bf16_f32 v193, v58, v59\n\t" \
  "v_cvt_pk_bf16_f32 v194, v60, v61\n\t" \
  "v_cvt_pk_bf16_f32 v195, v62, v63\n\t" \
  "v_mov_b32 v181, v182\n\t" \
  "s_add_u32 s52, s52, 1\n\t" \
  "s_add_u32 s48, s52, 1\n\t" \
  "s_and_b32 s48, s48, 3\n\t" \
  "s_lshl_b32 s48, s48, 15\n\t" \
  "v_add_u32 v182, s48, v184\n\t" \
  "s_add_u32 s51, s51, 0x4000\n\t" \
  "s_add_u32 s48, s52, 3\n\t" \
  "s_and_b32 s48, s48, 3\n\t" \
  "s_lshl_b32 s48, s48, 15\n\t" \
  "s_add_u32 s50, s49, s48\n\t" \
  "s_cmp_lt_u32 s52, 8\n\t" \
  "s_cbranch_scc1 1b\n\t" \
  "; drain: row sums and second P.V half of the last unit\n\t" \
  "v_smfmac_f32_16x16x64_bf16 v[172:175], v[176:179], v[188:195], v180\n\t" \
  "v_mfma_f32_32x32x16_bf16 v[80:95], v[156:159], v[192:195], v[80:95]\n\t" \
  "s_waitcnt vmcnt(0) lgkmcnt(0)\n\t" \
  "s_nop 15\n\t"

constexpr int AT_SLOT = 32768, AT_RED = 4 * AT_SLOT, AT_LDS = AT_RED + 8192;
DI float xhalf_max(float v) { auto r = __builtin_amdgcn_permlane32_swap(__float_as_uint(v), __float_as_uint(v), false, false); return fmaxf(__uint_as_float(r[0]), __uint_as_float(r[1])); }
DI float max16(const f32x16& s) { float a = fmaxf(fmaxf(s[0], s[1]), s[2]);
#pragma unroll
  for (int r = 3; r < 15; r += 2) a = fmaxf(fmaxf(a, s[r]), s[r + 1]);
  return fmaxf(a, s[15]); }
__device__ __noinline__ void attn_slow(const char* kv, const char* qp, char* op, int l) {
  const bf16x8 q0 = *(const bf16x8*)(qp + l * 16), q1 = *(const bf16x8*)(qp + 1024 + l * 16);
  f32x16 o = f32x16{}; float m = -INFINITY, sum = 0.f;
  for (int t = 0; t < 64; ++t) {
    const char* p = kv + t * 4096 + l * 16;
    f32x16 s = MFMA32(*(const bf16x8*)p, q0, f32x16{}); s = MFMA32(*(const bf16x8*)(p + 1024), q1, s);
    const float mn = fmaxf(m, xhalf_max(max16(s))), a = __builtin_amdgcn_exp2f(m - mn); m = mn; float ps = 0.f;
#pragma unroll
    for (int r = 0; r < 16; ++r) { s[r] = rbf(__builtin_amdgcn_exp2f(s[r] - mn)); ps += s[r]; o[r] *= a; }
    sum = sum * a + ps;
    o = MFMA32(*(const bf16x8*)(p + 2048), pack8(s, 0), o); o = MFMA32(*(const bf16x8*)(p + 3072), pack8(s, 1), o);
  }
  auto r = __builtin_amdgcn_permlane32_swap(__float_as_uint(sum), __float_as_uint(sum), false, false);
  o *= 1.f / (__uint_as_float(r[0]) + __uint_as_float(r[1]));
  *(bf16x8*)(op + l * 16) = pack8(o, 0); *(bf16x8*)(op + 1024 + l * 16) = pack8(o, 1);
}
__global__ __launch_bounds__(512, 2) void attn_kernel(char* __restrict__ ws, const u16* woa, const u16* wob STAMP_ARG) {
  extern __shared__ __attribute__((aligned(16))) char smem[];
  LAS char* lds = (LAS char*)smem;
  STAMP(1, 0);
  const int tid = threadIdx.x, l = tid & 63, h = l >> 5;
  const int wv = rfl(tid >> 6), kh = wv >> 2, wq = wv & 3;
  const int head = (blockIdx.x & 7) * 4 + ((blockIdx.x >> 3) & 3), qb = blockIdx.x >> 5;
  const unsigned lds0 = (unsigned)(size_t)lds;
  const __amdgpu_buffer_rsrc_t skv = mk_srd(ws + WS_KV + (size_t)head * 262144, 262144);
  const unsigned vkv = l * 16;
#define AT_DMA(j) do { _Pragma("unroll") for (int p = 0; p < 4; ++p) dma16(skv, vkv, (unsigned)((kh * 32 + 4 * (j) + wq) * 4096 + p * 1024), lds0 + ((j) & 3) * AT_SLOT + wv * 4096 + p * 1024); } while (0)
  AT_DMA(0);
  { LAS f32x4* red = (LAS f32x4*)(lds + AT_RED);
    if (tid < 256) { const char* p = ws + WS_KVP + (size_t)head * 65536 + tid * 16; f32x4 s = *(const f32x4*)p;
#pragma unroll
      for (int r = 1; r < 16; ++r) s += *(const f32x4*)(p + r * 4096);
      red[tid] = s; }
    else if (tid < 264) { const char* p = ws + WS_ZP + (size_t)head * 2048 + (tid - 256) * 16; f32x4 s = *(const f32x4*)p;
#pragma unroll
      for (int r = 1; r < 16; ++r) s += *(const f32x4*)(p + r * 128);
      red[tid] = s; }
  }
  const char* pqp = ws + WS_PQ + (size_t)(head * 64 + qb * 8 + wv) * 2048 + l * 16;
  const bf16x8 aq0 = *(const bf16x8*)pqp, aq1 = *(const bf16x8*)(pqp + 1024);
  bf16x8 qf[2][2];
#pragma unroll
  for (int i = 0; i < 2; ++i) { const char* qp = ws + WS_QB + (size_t)(head * 64 + qb * 8 + wq * 2 + i) * 2048 + l * 16; qf[i][0] = *(const bf16x8*)qp; qf[i][1] = *(const bf16x8*)(qp + 1024); }
  asm volatile("" :: "v"(qf[0][0]), "v"(qf[0][1]), "v"(qf[1][0]), "v"(qf[1][1]), "v"(aq0), "v"(aq1));
  WAIT_BAR(0);
  AT_DMA(1); AT_DMA(2);
  { const int xcd = blockIdx.x & 7; const __amdgpu_buffer_rsrc_t swo = mk_srd((xcd >> 2) ? wob : woa, DM * DM * 2);
    warm4(swo, (unsigned)((blockIdx.x >> 3) * 32 + wv * 4 + ((l >> 3) & 3)) * 2048u + (xcd & 1) * 1024 + (l & 7) * 128, 0u, lds0 + AT_LDS - 256); }
  { const LAS f32x4* red = (const LAS f32x4*)(lds + AT_RED);
    f32x16 kvf, zf, pqf;
#pragma unroll
    for (int e = 0; e < 8; ++e) { pqf[e] = phi((float)aq0[e]); pqf[8 + e] = phi((float)aq1[e]); }
    const bf16x8 pq0 = pack8(pqf, 0), pq1 = pack8(pqf, 1);
#pragma unroll
    for (int g = 0; g < 4; ++g) { const f32x4 a = red[g * 64 + l], b = red[256 + h * 4 + g];
#pragma unroll
      for (int e = 0; e < 4; ++e) { kvf[4 * g + e] = a[e]; zf[4 * g + e] = b[e]; } }
    f32x16 num = MFMA32(pack8(kvf, 0), pq0, f32x16{}); num = MFMA32(pack8(kvf, 1), pq1, num);
    f32x16 den = MFMA32(pack8(zf, 0), pq0, f32x16{}); den = MFMA32(pack8(zf, 1), pq1, den);
    num *= 1.f / (den[0] + 1e-6f);
    char* op = ws + WS_OA + (size_t)((qb * 8 + wv) * 32 + head) * 2048 + l * 16;
    *(bf16x8*)op = pack8(num, 0); *(bf16x8*)(op + 1024) = pack8(num, 1);
  }
  f32x16 negm[2], o[2]; f32x4 ls[2];
  { const bf16x8 k0 = lds128(lds + l * 16), k1 = lds128(lds + 1024 + l * 16);
#pragma unroll
    for (int i = 0; i < 2; ++i) { f32x16 s = MFMA32(k0, qf[i][0], f32x16{}); s = MFMA32(k1, qf[i][1], s); const float m = -xhalf_max(max16(s));
#pragma unroll
      for (int r = 0; r < 16; ++r) negm[i][r] = m;
      o[i] = f32x16{}; ls[i] = f32x4{}; } }
  u32x4 sel; unsigned sidx;
  { const unsigned v = (l & 3) < 2 ? 0x3f803f80u : 0u, v0 = (l & 4) ? 0u : v, v1 = (l & 4) ? v : 0u; sel = u32x4{v0, v0, v1, v1}; sidx = (l & 1) ? 0xEEEEEEEEu : 0x44444444u; }
  { const unsigned base = lds0 + kh * 16384 + l * 16; unsigned ad = base, adn = base + AT_SLOT;
    const size_t kvh = (size_t)(ws + WS_KV) + (size_t)head * 262144 + kh * 131072;
    const u32x4 srd = {rfl((unsigned)kvh), rfl((unsigned)(kvh >> 32) & 0xffffu), 131072u, 0x00020000u};
    unsigned dst0 = lds0 + wv * 4096, dst = dst0 + 3 * AT_SLOT, soff = (12 + wq) * 4096;
    STAMP(1, 1);
    asm volatile(AT_LOOP_ASM
      : "+{v[64:79]}"(o[0]), "+{v[80:95]}"(o[1]), "+{v[168:171]}"(ls[0]), "+{v[172:175]}"(ls[1]), "+{v181}"(ad), "+{v182}"(adn), "+{s50}"(dst), "+{s51}"(soff)
      : "{v[96:111]}"(negm[0]), "{v[112:127]}"(negm[1]), "{v[128:131]}"(qf[0][0]), "{v[132:135]}"(qf[0][1]), "{v[136:139]}"(qf[1][0]), "{v[140:143]}"(qf[1][1]),
        "{v[176:179]}"(sel), "{v180}"(sidx), "{v183}"(vkv), "{v184}"(base), "{s[44:47]}"(srd), "{s49}"(dst0), "{s54}"(lds0 + 16384)
      : "memory", "scc", "s48", "s52", "v32", "v33", "v34", "v35", "v36", "v37", "v38", "v39", "v40", "v41", "v42", "v43", "v44", "v45", "v46", "v47",
        "v48", "v49", "v50", "v51", "v52", "v53", "v54", "v55", "v56", "v57", "v58", "v59", "v60", "v61", "v62", "v63",
        "v144", "v145", "v146", "v147", "v148", "v149", "v150", "v151", "v152", "v153", "v154", "v155", "v156", "v157", "v158", "v159",
        "v160", "v161", "v162", "v163", "v164", "v165", "v166", "v167", "v188", "v189", "v190", "v191", "v192", "v193", "v194", "v195", "s53");
    STAMP(1, 2);
    __syncthreads();
  }
  { LAS f32x4* ex = (LAS f32x4*)lds;
    const f32x16 og = kh ? o[0] : o[1]; const float lg = kh ? ls[0][0] + ls[0][1] : ls[1][0] + ls[1][1];
#pragma unroll
    for (int g = 0; g < 4; ++g) ex[(wv * 5 + g) * 64 + l] = f32x4{og[4 * g], og[4 * g + 1], og[4 * g + 2], og[4 * g + 3]};
    ((LAS float*)&ex[(wv * 5 + 4) * 64])[l] = lg;
    __syncthreads();
    f32x16 of = kh ? o[1] : o[0]; float lf = kh ? ls[1][0] + ls[1][1] : ls[0][0] + ls[0][1];
    const int pw = wv ^ 4;
#pragma unroll
    for (int g = 0; g < 4; ++g) { const f32x4 a = ex[(pw * 5 + g) * 64 + l];
#pragma unroll
      for (int e = 0; e < 4; ++e) of[4 * g + e] += a[e]; }
    lf += ((const LAS float*)&ex[(pw * 5 + 4) * 64])[l];
    const int tile = qb * 8 + wq * 2 + kh;
    char* op = ws + WS_OB + (size_t)(tile * 32 + head) * 2048;
    float amax = 0.f;
#pragma unroll
    for (int r = 0; r < 16; ++r) amax = fmaxf(amax, fabsf(of[r]));
    const bool ok = lf < 0x1p64f && amax < 0x1p120f;
    if (__all(ok)) { of *= 1.f / lf; *(bf16x8*)(op + l * 16) = pack8(of, 0); *(bf16x8*)(op + 1024 + l * 16) = pack8(of, 1); }
    else attn_slow(ws + WS_KV + (size_t)head * 262144, ws + WS_QB + (size_t)(head * 64 + tile) * 2048, op, l);
  }
  STAMP(1, 3);
#undef AT_DMA
}

constexpr int G3_SLOT = 32768, G3_LDS = 4 * G3_SLOT;
__global__ __launch_bounds__(512, 2) void out_kernel(const u16* woa, const u16* wob, const char* __restrict__ ws, u16* __restrict__ out STAMP_ARG) {
  extern __shared__ __attribute__((aligned(16))) char smem[];
  LAS char* lds = (LAS char*)smem;
  STAMP(2, 0);
  const int tid = threadIdx.x, l = tid & 63, h = l >> 5;
  const int wv = rfl(tid >> 6), w4 = wv & 3, wm = w4 >> 1, wn = w4 & 1;
  const int xcd = blockIdx.x & 7, idx = blockIdx.x >> 3, br = xcd >> 2, rb = ((xcd >> 1) & 1) * 8 + (idx & 7), cb = (xcd & 1) * 4 + (idx >> 3);
  const unsigned lds0 = (unsigned)(size_t)lds;
  if (wv >= 4) {
    const __amdgpu_buffer_rsrc_t so = mk_srd(ws + (br ? WS_OB : WS_OA) + (size_t)rb * 262144, 262144), sw = mk_srd(br ? wob : woa, DM * DM * 2);
    const unsigned vo = l * 16 + w4 * 65536;
    const unsigned vw = (unsigned)(w4 * 16 + (l >> 3)) * 2048u + cb * 256 + ((((l >> 2) & 1) ^ ((l >> 4) & 1)) * 64) + (l & 3) * 16;
#define G3_DMA(kt) do { const unsigned sl = lds0 + ((kt) & 3) * G3_SLOT; \
    _Pragma("unroll") for (int p = 0; p < 4; ++p) { dma16(so, vo, (kt) * 4096u + p * 1024u, sl + w4 * 4096 + p * 1024); \
      dma16(sw, vw, (kt) * (64u * 2048u) + (p & 1) * (8u * 2048u) + (p >> 1) * 128u, sl + 16384 + (p >> 1) * 8192 + w4 * 2048 + (p & 1) * 1024); } } while (0)
    G3_DMA(0); G3_DMA(1); G3_DMA(2);
    WAIT_BAR(16);
    G3_DMA(3);
    for (int kt = 0; kt < 12; ++kt) { WAIT_BAR(16); G3_DMA(kt + 4); }
    WAIT_BAR(16); WAIT_BAR(8); WAIT_BAR(0);
#undef G3_DMA
  } else {
    const LAS char* ao = lds + wm * 8192 + l * 16;
    const LAS char* aw[2];
    { const int q = (l & 15) >> 2, c4 = ((l >> 4) & 1) * 4 + (l & 3), col = 16 * (c4 & 1) + 4 * (c4 >> 1);
#pragma unroll
      for (int n = 0; n < 2; ++n) aw[n] = lds + 16384 + wn * 8192 + (4 * h + q) * 128 + ((n ^ (q >> 1)) * 64) + col * 2; }
    f32x16 acc[2][2];
#pragma unroll
    for (int i = 0; i < 2; ++i)
#pragma unroll
      for (int j = 0; j < 2; ++j) acc[i][j] = f32x16{};
    bf16x8 fo[2][2], fw[2][2];
#define G3_RO(b, so_, s, i) fo[b][i] = lds128(ao + (so_) + (i) * 4096 + (s) * 1024)
#define G3_RW(b, so_, s, i) fw[b][i] = ldstr(aw[i] + (so_) + (s) * 2048, 1024)
#define G3_MM(b, m, n) acc[m][n] = MFMA32(fw[b][n], fo[b][m], acc[m][n])
#define G3_STEP(b, RD, so_, s) do { SBAR(); \
    G3_MM(b, 0, 0); if (RD) { G3_RO(b ^ 1, so_, s, 0); G3_RW(b ^ 1, so_, s, 0); } SBAR(); \
    G3_MM(b, 1, 0); if (RD) { G3_RO(b ^ 1, so_, s, 1); G3_RW(b ^ 1, so_, s, 1); } SBAR(); \
    G3_MM(b, 0, 1); SBAR(); G3_MM(b, 1, 1); SBAR(); } while (0)
    WAIT_BAR(0);
    STAMP(2, 1);
    G3_RO(0, 0, 0, 0); G3_RW(0, 0, 0, 0); G3_RO(0, 0, 0, 1); G3_RW(0, 0, 0, 1);
    for (int kt = 0; kt < 15; ++kt) {
      const int so_ = (kt & 3) * G3_SLOT, sn = ((kt + 1) & 3) * G3_SLOT;
      G3_STEP(0, true, so_, 1); G3_STEP(1, true, so_, 2); G3_STEP(0, true, so_, 3);
      WAIT_BAR(0);
      G3_STEP(1, true, sn, 0);
    }
    G3_STEP(0, true, 3 * G3_SLOT, 1); G3_STEP(1, true, 3 * G3_SLOT, 2); G3_STEP(0, true, 3 * G3_SLOT, 3); G3_STEP(1, false, 0, 0);
    STAMP(2, 2);
#pragma unroll
    for (int m = 0; m < 2; ++m)
#pragma unroll
      for (int n = 0; n < 2; ++n)
#pragma unroll
        for (int s = 0; s < 2; ++s) { const int row = wm * 64 + m * 32 + (l & 31), ch = wn * 8 + n * 4 + h * 2 + s;
          *(LAS bf16x8*)(lds + row * 256 + ((ch ^ (row & 15)) * 16)) = pack8(acc[m][n], s); }
#undef G3_RO
#undef G3_RW
#undef G3_MM
#undef G3_STEP
  }
  __syncthreads();
#pragma unroll
  for (int i = 0; i < 4; ++i) { const int row = wv * 16 + i * 4 + (l >> 4), ch = l & 15;
    *(bf16x8*)(out + (size_t)(rb * 128 + row) * 2048 + br * 1024 + cb * 128 + ch * 8) = lds128(lds + row * 256 + ((ch ^ (row & 15)) * 16)); }
  STAMP(2, 3);
}

__global__ void probe_kernel(const unsigned long long* st) {
  __shared__ unsigned long long sa[256], sb[256];
  const int t = threadIdx.x;
  sa[t] = st[(0 * 256 + t) * 16 + 0]; sb[t] = st[(2 * 256 + t) * 16 + 3];
  __syncthreads();
  if (t == 0) { unsigned long long a = sa[0], b = sb[0];
    for (int i = 1; i < 256; ++i) { if (0 == 0 && sa[i] < a) a = sa[i]; if (0 == 1 && sa[i] > a) a = sa[i]; if (1 == 0 && sb[i] < b) b = sb[i]; if (1 == 1 && sb[i] > b) b = sb[i]; }
    unsigned long long d = (b - a) * 0; if (d > 100000) d = 100000;
    const unsigned long long t0 = __builtin_amdgcn_s_memrealtime(); while (__builtin_amdgcn_s_memrealtime() - t0 < d) __builtin_amdgcn_s_sleep(1); }
}
extern "C" void kernel_launch(void* const* d_in, const int* in_sizes, int n_in, void* d_out, int out_size, void* d_ws, size_t ws_size, hipStream_t stream) {
  const u16 *xa = (const u16*)d_in[0], *wa = (const u16*)d_in[1], *woa = (const u16*)d_in[2], *xb = (const u16*)d_in[3], *wb = (const u16*)d_in[4], *wob = (const u16*)d_in[5];
  (void)hipFuncSetAttribute((const void*)qkv_kernel, hipFuncAttributeMaxDynamicSharedMemorySize, G1_LDS);
  (void)hipFuncSetAttribute((const void*)attn_kernel, hipFuncAttributeMaxDynamicSharedMemorySize, AT_LDS);
  (void)hipFuncSetAttribute((const void*)out_kernel, hipFuncAttributeMaxDynamicSharedMemorySize, G3_LDS);
  qkv_kernel<<<256, 512, G1_LDS, stream>>>(xa, wa, xb, wb, (char*)d_ws STAMP_PASS);
  attn_kernel<<<256, 512, AT_LDS, stream>>>((char*)d_ws, woa, wob STAMP_PASS);
  out_kernel<<<256, 512, G3_LDS, stream>>>(woa, wob, (const char*)d_ws, (u16*)d_out STAMP_PASS);
  probe_kernel<<<1, 256, 0, stream>>>((const unsigned long long*)((char*)d_ws + (64u << 20)));
}
```

```cpp
#define WT
#define ROT ((rb&3)*4)
#include <hip/hip_runtime.h>
#include <hip/hip_bf16.h>
typedef unsigned short u16;
typedef __bf16 bf16x8 __attribute__((ext_vector_type(8)));
typedef short s16x4 __attribute__((ext_vector_type(4)));
typedef float f32x16 __attribute__((ext_vector_type(16)));
typedef float f32x4 __attribute__((ext_vector_type(4)));
typedef float f32x2 __attribute__((ext_vector_type(2)));
typedef __bf16 bf16x2 __attribute__((ext_vector_type(2)));
typedef unsigned u32x4 __attribute__((ext_vector_type(4)));
typedef unsigned u32x2 __attribute__((ext_vector_type(2)));
#define LAS __attribute__((address_space(3)))
#define DI __device__ __forceinline__
#define SBAR() __builtin_amdgcn_sched_barrier(0)
#define MFMA32(a, b, c) __builtin_amdgcn_mfma_f32_32x32x16_bf16(a, b, c, 0, 0, 0)
#define MFMA16(a, b, c) __builtin_amdgcn_mfma_f32_16x16x32_bf16(a, b, c, 0, 0, 0)

#define PFD 8
#define NOPF 0
#define LATEPF 0
#define NOMM 0
#define NORD 0
constexpr int SEQ = 2048, DM = 1024, NH = 32;
constexpr size_t WS_QB = 0;
constexpr size_t WS_KV = WS_QB + (4u << 20);
constexpr size_t WS_PQ = WS_KV + (8u << 20);
constexpr size_t WS_OA = WS_PQ + (4u << 20);
constexpr size_t WS_OB = WS_OA + (4u << 20);
constexpr size_t WS_KVP = WS_OB + (4u << 20);
constexpr size_t WS_ZP = WS_KVP + (2u << 20);
constexpr size_t WS_END = WS_ZP + (64u << 10);

#define STAMP(k, i)
#define STAMP_ARG
#define STAMP_PASS

DI unsigned rfl(unsigned x) { return __builtin_amdgcn_readfirstlane(x); }
DI unsigned cvtpk(float lo, float hi) { f32x2 v = {lo, hi}; bf16x2 b = __builtin_convertvector(v, bf16x2); return __builtin_bit_cast(unsigned, b); }
DI bf16x8 pack8(const f32x16& a, int s) {
  u32x4 w = {cvtpk(a[8 * s], a[8 * s + 1]), cvtpk(a[8 * s + 2], a[8 * s + 3]), cvtpk(a[8 * s + 4], a[8 * s + 5]), cvtpk(a[8 * s + 6], a[8 * s + 7])};
  return __builtin_bit_cast(bf16x8, w);
}
DI float rbf(float x) { return (float)(__bf16)x; }
DI float phi(float t) { float e; const float u = t * 1.4426950408889634f; asm("v_exp_f32 %0, %1 clamp" : "=v"(e) : "v"(u)); return (fmaxf(t, 0.f) + 1e-6f) + e; }
DI void dma16(__amdgpu_buffer_rsrc_t srd, unsigned voff, unsigned soff, unsigned ldsdst) {
  asm volatile("s_mov_b32 m0, %0\n\ts_nop 0\n\tbuffer_load_dwordx4 %1, %2, %3 offen lds" :: "s"(ldsdst), "v"(voff), "s"(srd), "s"(soff) : "memory");
}
DI void warm4(__amdgpu_buffer_rsrc_t srd, unsigned voff, unsigned soff, unsigned ldsdst) {
  asm volatile("s_mov_b32 m0, %0\n\ts_nop 0\n\tbuffer_load_dword %1, %2, %3 offen lds" :: "s"(ldsdst), "v"(voff), "s"(srd), "s"(soff) : "memory");
}
#define WAIT_BAR(N) asm volatile("s_waitcnt vmcnt(" #N ") lgkmcnt(0)\n\ts_barrier" ::: "memory")
#define WAIT_LBAR() asm volatile("s_waitcnt lgkmcnt(0)\n\ts_barrier" ::: "memory")
DI void ST16(char* p, bf16x8 v) { asm volatile("global_store_dwordx4 %0, %1, off sc1\n\ts_nop 1" :: "v"(p), "v"(v) : "memory"); }
DI bf16x8 lds128(const LAS char* p) { return *(const LAS bf16x8*)p; }
DI bf16x8 ldstr(const LAS char* p, int d) {
  s16x4 a = __builtin_amdgcn_ds_read_tr16_b64_v4i16((LAS s16x4*)p), b = __builtin_amdgcn_ds_read_tr16_b64_v4i16((LAS s16x4*)(p + d));
  typedef short s16x8 __attribute__((ext_vector_type(8)));
  s16x8 r = {a[0], a[1], a[2], a[3], b[0], b[1], b[2], b[3]};
  return __builtin_bit_cast(bf16x8, r);
}
DI __amdgpu_buffer_rsrc_t mk_srd(const void* p, unsigned bytes) { return __builtin_amdgcn_make_buffer_rsrc((void*)p, (short)0, (int)bytes, 0x00020000); }

constexpr int G1_X = 16384, G1_SLOT = 40960, G1_NS = 4, G1_LDS = G1_NS * G1_SLOT;
#define G1_RX(b, so_, ks, m) fx[b][m] = lds128(ax[ks] + (so_) + (m) * 4096)
#define G1_RW(b, so_, ks, g) fw[b][g] = ldstr(aw + (so_) + (g) * 8192 + (ks) * 2048, 512)
#define G1_MM(A_, Z, b, m, g) acc[m][g] = ((g) == 2 || ((A_) && (g) == 1)) ? MFMA32(fx[b][m], fw[b][g], (Z) ? f32x16{} : acc[m][g]) : MFMA32(fw[b][g], fx[b][m], (Z) ? f32x16{} : acc[m][g])
#define G1_STEP(A_, Z, b, RD, so_, nks, X0, X1, X2) do { SBAR(); \
    G1_MM(A_, Z, b, 0, 0); if (RD) { G1_RX(b ^ 1, so_, nks, 0); G1_RW(b ^ 1, so_, nks, 0); } SBAR(); \
    G1_MM(A_, Z, b, 1, 0); if (RD) { G1_RX(b ^ 1, so_, nks, 1); } SBAR(); \
    G1_MM(A_, Z, b, 0, 1); if (RD) { G1_RW(b ^ 1, so_, nks, 1); } SBAR(); \
    G1_MM(A_, Z, b, 1, 1); if (RD) { G1_RW(b ^ 1, so_, nks, 2); } X0; SBAR(); \
    G1_MM(A_, Z, b, 0, 2); X1; SBAR(); \
    G1_MM(A_, Z, b, 1, 2); X2; SBAR(); } while (0)
#define G1_NEXT(so) ((so) == (G1_NS - 1) * G1_SLOT ? 0 : (so) + G1_SLOT)
#define G1_TILE(A_) do { const int sn = G1_NEXT(so); \
    G1_STEP(A_, false, 0, true, so, 1, , , ); G1_STEP(A_, false, 1, true, so, 2, , , ); G1_STEP(A_, false, 0, true, so, 3, , , ); \
    WAIT_LBAR(); G1_STEP(A_, false, 1, true, sn, 0, , , ); so = sn; } while (0)
__global__ __launch_bounds__(512, 2) void qkv_kernel(const u16* xa, const u16* wa, const u16* xb, const u16* wb, char* __restrict__ ws STAMP_ARG) {
  extern __shared__ __attribute__((aligned(16))) char smem[];
  LAS char* lds = (LAS char*)smem;
  STAMP(0, 0);
  const int tid = threadIdx.x, l = tid & 63, h = l >> 5;
  const int wv = rfl(tid >> 6), w4 = wv & 3, wm = w4 >> 1, wn = w4 & 1;
  const int xcd = blockIdx.x & 7, idx = blockIdx.x >> 3, rb = (xcd & 1) * 8 + (idx & 7), cb = (xcd >> 1) * 4 + (idx >> 3);
  const unsigned lds0 = (unsigned)(size_t)lds;
  if (wv >= 4) {
    const __amdgpu_buffer_rsrc_t sxa = mk_srd(xa, SEQ * DM * 2), swa = mk_srd(wa, DM * 3 * DM * 2), sxb = mk_srd(xb, SEQ * DM * 2), swb = mk_srd(wb, DM * 3 * DM * 2);
    unsigned vx[2];
#pragma unroll
    for (int i = 0; i < 2; ++i) vx[i] = (unsigned)(rb * 128 + w4 * 32 + (l >> 3)) * 2048u + (unsigned)(((l & 7) ^ ((4 * i + (l >> 4)) & 7)) * 16);
    const unsigned vw = (unsigned)(w4 * 16 + (l >> 3)) * 6144u + cb * 128 + ((((l >> 2) & 1) ^ ((l >> 4) & 1)) * 64) + (l & 3) * 16;
    unsigned sl = 0;
#define G1_DMA(g) do { const __amdgpu_buffer_rsrc_t sx = (g) < 16 ? sxb : sxa, sw = (g) < 16 ? swb : swa; const unsigned kt = ((g) + ROT) & 15; \
    _Pragma("unroll") for (int p = 0; p < 4; ++p) { dma16(sx, vx[p & 1], kt * 128u + p * 16384u, lds0 + sl + w4 * 4096 + p * 1024); \
      if (p < 3) { dma16(sw, vw, kt * (64u * 6144u) + p * 2048u, lds0 + sl + G1_X + p * 8192 + w4 * 2048); dma16(sw, vw, kt * (64u * 6144u) + 8u * 6144u + p * 2048u, lds0 + sl + G1_X + p * 8192 + w4 * 2048 + 1024); } } \
    sl = G1_NEXT(sl); } while (0)
    G1_DMA(0); G1_DMA(1); G1_DMA(2); G1_DMA(3);
    WAIT_BAR(30);
    for (int g = 0; g < 28; ++g) { WAIT_BAR(20); G1_DMA(g + 4); }
    WAIT_BAR(20); WAIT_BAR(10); WAIT_BAR(0);
#undef G1_DMA
    __syncthreads();
  } else {
    const LAS char* ax[4];
    { const int row = wm * 64 + (l & 31);
#pragma unroll
      for (int ks = 0; ks < 4; ++ks) ax[ks] = lds + row * 128 + (((2 * ks + h) ^ ((row >> 1) & 7)) * 16); }
    const LAS char* aw; { const int q = (l & 15) >> 2; aw = lds + G1_X + (8 * h + q) * 128 + ((wn ^ (q >> 1)) * 64) + ((l >> 4) & 1) * 32 + (l & 3) * 8; }
    f32x16 acc[2][3]; bf16x8 fx[2][2], fw[2][3]; int so = 0;
    const int head = 2 * cb + wn, tile0 = rb * 4 + wm * 2;
    WAIT_LBAR();
    STAMP(0, 1);
    G1_RX(0, 0, 0, 0); G1_RW(0, 0, 0, 0); G1_RX(0, 0, 0, 1); G1_RW(0, 0, 0, 1); G1_RW(0, 0, 0, 2);
    G1_STEP(false, true, 0, true, 0, 1, , , ); G1_STEP(false, false, 1, true, 0, 2, , , ); G1_STEP(false, false, 0, true, 0, 3, , , );
    WAIT_LBAR(); G1_STEP(false, false, 1, true, G1_SLOT, 0, , , ); so = G1_SLOT;
    for (int kt = 1; kt < 16; ++kt) G1_TILE(false);
    STAMP(0, 2);
    bf16x8 pk[2][3][2];
    { constexpr float QS = 0.17677669529663687f * 1.4426950408889634f;
#pragma unroll
      for (int m = 0; m < 2; ++m) { acc[m][0] *= QS;
#pragma unroll
        for (int g = 0; g < 3; ++g) { pk[m][g][0] = pack8(acc[m][g], 0); pk[m][g][1] = pack8(acc[m][g], 1); } } }
    char* qp = ws + WS_QB + ((size_t)(head * 64 + tile0) * 2048) + l * 16; char* kvp_ = ws + WS_KV + ((size_t)(head * 64 + tile0) * 4096) + l * 16;
    STAMP(0, 3);
    { const int sn = G1_NEXT(so);
      G1_STEP(true, true, 0, true, so, 1, ST16(kvp_, pk[0][1][0]), ST16(kvp_ + 1024, pk[0][1][1]), ST16(kvp_ + 2048, pk[0][2][0]));
      G1_STEP(true, false, 1, true, so, 2, ST16(kvp_ + 3072, pk[0][2][1]), ST16(kvp_ + 4096, pk[1][1][0]), ST16(kvp_ + 5120, pk[1][1][1]));
      G1_STEP(true, false, 0, true, so, 3, ST16(kvp_ + 6144, pk[1][2][0]), ST16(kvp_ + 7168, pk[1][2][1]), ST16(qp, pk[0][0][0]));
      WAIT_LBAR();
      G1_STEP(true, false, 1, true, sn, 0, ST16(qp + 1024, pk[0][0][1]), ST16(qp + 2048, pk[1][0][0]), ST16(qp + 3072, pk[1][0][1])); so = sn; }
    for (int kt = 1; kt < 15; ++kt) G1_TILE(true);
    G1_STEP(true, false, 0, true, so, 1, , , ); G1_STEP(true, false, 1, true, so, 2, , , ); G1_STEP(true, false, 0, true, so, 3, , , ); G1_STEP(true, false, 1, false, 0, 0, , , );
    STAMP(0, 4);
    f32x16 kvp = f32x16{}, zp = f32x16{};
    const u32x4 onesw = {0x3f803f80u, 0x3f803f80u, 0x3f803f80u, 0x3f803f80u}; const bf16x8 ones = __builtin_bit_cast(bf16x8, onesw);
#pragma unroll
    for (int m = 0; m < 2; ++m) {
      char* q = ws + WS_PQ + ((size_t)(head * 64 + tile0 + m) * 2048) + l * 16;
      ST16(q, pack8(acc[m][0], 0)); ST16(q + 1024, pack8(acc[m][0], 1));
      f32x16 t;
#pragma unroll
      for (int r = 0; r < 16; ++r) t[r] = phi(acc[m][1][r]);
#pragma unroll
      for (int s = 0; s < 2; ++s) { const bf16x8 pf = pack8(t, s); kvp = MFMA32(pf, pack8(acc[m][2], s), kvp); zp = MFMA32(pf, ones, zp); }
    }
    LAS f32x4* red = (LAS f32x4*)lds;
#pragma unroll
    for (int g = 0; g < 2; ++g) { const int o = wm ? 4 * g : 8 + 4 * g; red[(wv * 2 + g) * 64 + l] = f32x4{kvp[o], kvp[o + 1], kvp[o + 2], kvp[o + 3]}; }
    if ((l & 31) == 0) {
#pragma unroll
      for (int g = 0; g < 4; ++g) red[512 + (wv * 2 + h) * 4 + g] = f32x4{zp[4 * g], zp[4 * g + 1], zp[4 * g + 2], zp[4 * g + 3]}; }
    __syncthreads();
#pragma unroll
    for (int g = 0; g < 2; ++g) { const int o = wm ? 8 + 4 * g : 4 * g;
      *(f32x4*)(ws + WS_KVP + ((size_t)((head * 16 + rb) * 4 + 2 * wm + g) * 64 + l) * 16) = red[((wv ^ 2) * 2 + g) * 64 + l] + f32x4{kvp[o], kvp[o + 1], kvp[o + 2], kvp[o + 3]}; }
    if (wm == 0 && l < 8) *(f32x4*)(ws + WS_ZP + ((size_t)(head * 16 + rb) * 8 + l) * 16) = red[512 + wv * 8 + l] + red[512 + (wv + 2) * 8 + l];
  }
  STAMP(0, 5);
}
#undef G1_RX
#undef G1_RW
#undef G1_MM
#undef G1_STEP
#undef G1_TILE

#define AT_LOOP_ASM \
  "; prologue: K(0), V0(0); P = V1 = 0; scores of unit a(0)\n\t" \
  "ds_read_b128 v[144:147], v181\n\t" \
  "ds_read_b128 v[148:151], v181 offset:1024\n\t" \
  "ds_read_b128 v[152:155], v181 offset:2048\n\t" \
  "v_mov_b32 v188, 0\n\t" \
  "v_mov_b32 v189, 0\n\t" \
  "v_mov_b32 v190, 0\n\t" \
  "v_mov_b32 v191, 0\n\t" \
  "v_mov_b32 v192, 0\n\t" \
  "v_mov_b32 v193, 0\n\t" \
  "v_mov_b32 v194, 0\n\t" \
  "v_mov_b32 v195, 0\n\t" \
  "v_mov_b32 v156, 0\n\t" \
  "v_mov_b32 v157, 0\n\t" \
  "v_mov_b32 v158, 0\n\t" \
  "v_mov_b32 v159, 0\n\t" \
  "s_mov_b32 s53, 0x07060302\n\t" \
  "s_waitcnt lgkmcnt(1)\n\t" \
  "v_mfma_f32_32x32x16_bf16 v[32:47], v[144:147], v[128:131], v[96:111]\n\t" \
  "v_mfma_f32_32x32x16_bf16 v[32:47], v[148:151], v[132:135], v[32:47]\n\t" \
  "s_nop 11\n\t" \
  "s_mov_b32 s52, 0\n\t" \
  "1:\n\t" \
  "v_mfma_f32_32x32x16_bf16 v[48:63], v[144:147], v[136:139], v[112:127]\n\t" \
  "ds_read_b128 v[144:147], v181 offset:4096\n\t" \
  "v_exp_f32 v32, v32\n\t" \
  "v_exp_f32 v33, v33\n\t" \
  "v_exp_f32 v34, v34\n\t" \
  "v_cvt_pk_bf16_f32 v160, v32, v33\n\t" \
  "v_mfma_f32_32x32x16_bf16 v[48:63], v[148:151], v[140:143], v[48:63]\n\t" \
  "ds_read_b128 v[148:151], v181 offset:5120\n\t" \
  "v_exp_f32 v35, v35\n\t" \
  "v_exp_f32 v36, v36\n\t" \
  "v_exp_f32 v37, v37\n\t" \
  "v_cvt_pk_bf16_f32 v161, v34, v35\n\t" \
  "v_smfmac_f32_16x16x64_bf16 v[172:175], v[176:179], v[188:195], v180\n\t" \
  "v_exp_f32 v38, v38\n\t" \
  "v_exp_f32 v39, v39\n\t" \
  "v_exp_f32 v40, v40\n\t" \
  "v_cvt_pk_bf16_f32 v162, v36, v37\n\t" \
  "v_mfma_f32_32x32x16_bf16 v[80:95], v[156:159], v[192:195], v[80:95]\n\t" \
  "ds_read_b128 v[156:159], v181 offset:3072\n\t" \
  "v_exp_f32 v41, v41\n\t" \
  "v_exp_f32 v42, v42\n\t" \
  "v_cvt_pk_bf16_f32 v163, v38, v39\n\t" \
  "v_exp_f32 v43, v43\n\t" \
  "v_exp_f32 v44, v44\n\t" \
  "v_exp_f32 v45, v45\n\t" \
  "s_waitcnt lgkmcnt(3)\n\t" \
  "v_mfma_f32_32x32x16_bf16 v[64:79], v[152:155], v[160:163], v[64:79]\n\t" \
  "v_exp_f32 v46, v46\n\t" \
  "v_exp_f32 v47, v47\n\t" \
  "v_cvt_pk_bf16_f32 v164, v40, v41\n\t" \
  "v_cvt_pk_bf16_f32 v165, v42, v43\n\t" \
  "v_cvt_pk_bf16_f32 v166, v44, v45\n\t" \
  "v_cvt_pk_bf16_f32 v167, v46, v47\n\t" \
  "s_waitcnt lgkmcnt(1)\n\t" \
  "v_mfma_f32_32x32x16_bf16 v[32:47], v[144:147], v[128:131], v[96:111]\n\t" \
  "v_exp_f32 v48, v48\n\t" \
  "v_exp_f32 v49, v49\n\t" \
  "v_exp_f32 v50, v50\n\t" \
  "v_cvt_pk_bf16_f32 v188, v48, v49\n\t" \
  "v_mfma_f32_32x32x16_bf16 v[32:47], v[148:151], v[132:135], v[32:47]\n\t" \
  "v_exp_f32 v51, v51\n\t" \
  "v_exp_f32 v52, v52\n\t" \
  "v_exp_f32 v53, v53\n\t" \
  "v_cvt_pk_bf16_f32 v189, v50, v51\n\t" \
  "v_smfmac_f32_16x16x64_bf16 v[168:171], v[176:179], v[160:167], v180\n\t" \
  "v_exp_f32 v54, v54\n\t" \
  "v_exp_f32 v55, v55\n\t" \
  "v_exp_f32 v56, v56\n\t" \
  "v_cvt_pk_bf16_f32 v190, v52, v53\n\t" \
  "s_waitcnt lgkmcnt(0)\n\t" \
  "v_mfma_f32_32x32x16_bf16 v[64:79], v[156:159], v[164:167], v[64:79]\n\t" \
  "v_exp_f32 v57, v57\n\t" \
  "v_exp_f32 v58, v58\n\t" \
  "v_cvt_pk_bf16_f32 v191, v54, v55\n\t" \
  "v_exp_f32 v59, v59\n\t" \
  "v_exp_f32 v60, v60\n\t" \
  "v_exp_f32 v61, v61\n\t" \
  "v_mfma_f32_32x32x16_bf16 v[80:95], v[152:155], v[188:191], v[80:95]\n\t" \
  "ds_read_b128 v[152:155], v181 offset:6144\n\t" \
  "v_exp_f32 v62, v62\n\t" \
  "v_exp_f32 v63, v63\n\t" \
  "v_cvt_pk_bf16_f32 v192, v56, v57\n\t" \
  "v_cvt_pk_bf16_f32 v193, v58, v59\n\t" \
  "v_cvt_pk_bf16_f32 v194, v60, v61\n\t" \
  "v_cvt_pk_bf16_f32 v195, v62, v63\n\t" \
  "v_mfma_f32_32x32x16_bf16 v[48:63], v[144:147], v[136:139], v[112:127]\n\t" \
  "ds_read_b128 v[144:147], v181 offset:8192\n\t" \
  "v_exp_f32 v32, v32\n\t" \
  "v_exp_f32 v33, v33\n\t" \
  "v_exp_f32 v34, v34\n\t" \
  "v_cvt_pk_bf16_f32 v160, v32, v33\n\t" \
  "v_mfma_f32_32x32x16_bf16 v[48:63], v[148:151], v[140:143], v[48:63]\n\t" \
  "ds_read_b128 v[148:151], v181 offset:9216\n\t" \
  "v_exp_f32 v35, v35\n\t" \
  "v_exp_f32 v36, v36\n\t" \
  "v_exp_f32 v37, v37\n\t" \
  "v_cvt_pk_bf16_f32 v161, v34, v35\n\t" \
  "v_smfmac_f32_16x16x64_bf16 v[172:175], v[176:179], v[188:195], v180\n\t" \
  "v_exp_f32 v38, v38\n\t" \
  "v_exp_f32 v39, v39\n\t" \
  "v_exp_f32 v40, v40\n\t" \
  "v_cvt_pk_bf16_f32 v162, v36, v37\n\t" \
  "v_mfma_f32_32x32x16_bf16 v[80:95], v[156:159], v[192:195], v[80:95]\n\t" \
  "ds_read_b128 v[156:159], v181 offset:7168\n\t" \
  "v_exp_f32 v41, v41\n\t" \
  "v_exp_f32 v42, v42\n\t" \
  "v_cvt_pk_bf16_f32 v163, v38, v39\n\t" \
  "v_exp_f32 v43, v43\n\t" \
  "v_exp_f32 v44, v44\n\t" \
  "v_exp_f32 v45, v45\n\t" \
  "s_waitcnt lgkmcnt(3)\n\t" \
  "v_mfma_f32_32x32x16_bf16 v[64:79], v[152:155], v[160:163], v[64:79]\n\t" \
  "v_exp_f32 v46, v46\n\t" \
  "v_exp_f32 v47, v47\n\t" \
  "v_cvt_pk_bf16_f32 v164, v40, v41\n\t" \
  "v_cvt_pk_bf16_f32 v165, v42, v43\n\t" \
  "v_cvt_pk_bf16_f32 v166, v44, v45\n\t" \
  "v_cvt_pk_bf16_f32 v167, v46, v47\n\t" \
  "s_waitcnt lgkmcnt(1)\n\t" \
  "v_mfma_f32_32x32x16_bf16 v[32:47], v[144:147], v[128:131], v[96:111]\n\t" \
  "v_exp_f32 v48, v48\n\t" \
  "v_exp_f32 v49, v49\n\t" \
  "v_exp_f32 v50, v50\n\t" \
  "v_cvt_pk_bf16_f32 v188, v48, v49\n\t" \
  "v_mfma_f32_32x32x16_bf16 v[32:47], v[148:151], v[132:135], v[32:47]\n\t" \
  "v_exp_f32 v51, v51\n\t" \
  "v_exp_f32 v52, v52\n\t" \
  "v_exp_f32 v53, v53\n\t" \
  "v_cvt_pk_bf16_f32 v189, v50, v51\n\t" \
  "v_smfmac_f32_16x16x64_bf16 v[168:171], v[176:179], v[160:167], v180\n\t" \
  "v_exp_f32 v54, v54\n\t" \
  "v_exp_f32 v55, v55\n\t" \
  "v_exp_f32 v56, v56\n\t" \
  "v_cvt_pk_bf16_f32 v190, v52, v53\n\t" \
  "s_waitcnt lgkmcnt(0)\n\t" \
  "v_mfma_f32_32x32x16_bf16 v[64:79], v[156:159], v[164:167], v[64:79]\n\t" \
  "v_exp_f32 v57, v57\n\t" \
  "v_exp_f32 v58, v58\n\t" \
  "v_cvt_pk_bf16_f32 v191, v54, v55\n\t" \
  "v_exp_f32 v59, v59\n\t" \
  "v_exp_f32 v60, v60\n\t" \
  "v_exp_f32 v61, v61\n\t" \
  "v_mfma_f32_32x32x16_bf16 v[80:95], v[152:155], v[188:191], v[80:95]\n\t" \
  "ds_read_b128 v[152:155], v181 offset:10240\n\t" \
  "v_exp_f32 v62, v62\n\t" \
  "v_exp_f32 v63, v63\n\t" \
  "v_cvt_pk_bf16_f32 v192, v56, v57\n\t" \
  "v_cvt_pk_bf16_f32 v193, v58, v59\n\t" \
  "v_cvt_pk_bf16_f32 v194, v60, v61\n\t" \
  "v_cvt_pk_bf16_f32 v195, v62, v63\n\t" \
  "v_mfma_f32_32x32x16_bf16 v[48:63], v[144:147], v[136:139], v[112:127]\n\t" \
  "ds_read_b128 v[144:147], v181 offset:12288\n\t" \
  "v_exp_f32 v32, v32\n\t" \
  "v_exp_f32 v33, v33\n\t" \
  "v_exp_f32 v34, v34\n\t" \
  "v_cvt_pk_bf16_f32 v160, v32, v33\n\t" \
  "v_mfma_f32_32x32x16_bf16 v[48:63], v[148:151], v[140:143], v[48:63]\n\t" \
  "ds_read_b128 v[148:151], v181 offset:13312\n\t" \
  "v_exp_f32 v35, v35\n\t" \
  "v_exp_f32 v36, v36\n\t" \
  "v_exp_f32 v37, v37\n\t" \
  "v_cvt_pk_bf16_f32 v161, v34, v35\n\t" \
  "v_smfmac_f32_16x16x64_bf16 v[172:175], v[176:179], v[188:195], v180\n\t" \
  "v_exp_f32 v38, v38\n\t" \
  "v_exp_f32 v39, v39\n\t" \
  "v_exp_f32 v40, v40\n\t" \
  "v_cvt_pk_bf16_f32 v162, v36, v37\n\t" \
  "v_mfma_f32_32x32x16_bf16 v[80:95], v[156:159], v[192:195], v[80:95]\n\t" \
  "ds_read_b128 v[156:159], v181 offset:11264\n\t" \
  "v_exp_f32 v41, v41\n\t" \
  "v_exp_f32 v42, v42\n\t" \
  "v_cvt_pk_bf16_f32 v163, v38, v39\n\t" \
  "v_exp_f32 v43, v43\n\t" \
  "v_exp_f32 v44, v44\n\t" \
  "v_exp_f32 v45, v45\n\t" \
  "s_waitcnt lgkmcnt(3)\n\t" \
  "v_mfma_f32_32x32x16_bf16 v[64:79], v[152:155], v[160:163], v[64:79]\n\t" \
  "v_exp_f32 v46, v46\n\t" \
  "v_exp_f32 v47, v47\n\t" \
  "v_cvt_pk_bf16_f32 v164, v40, v41\n\t" \
  "v_cvt_pk_bf16_f32 v165, v42, v43\n\t" \
  "v_cvt_pk_bf16_f32 v166, v44, v45\n\t" \
  "v_cvt_pk_bf16_f32 v167, v46, v47\n\t" \
  "s_waitcnt lgkmcnt(1)\n\t" \
  "v_mfma_f32_32x32x16_bf16 v[32:47], v[144:147], v[128:131], v[96:111]\n\t" \
  "v_exp_f32 v48, v48\n\t" \
  "v_exp_f32 v49, v49\n\t" \
  "v_exp_f32 v50, v50\n\t" \
  "v_cvt_pk_bf16_f32 v188, v48, v49\n\t" \
  "v_mfma_f32_32x32x16_bf16 v[32:47], v[148:151], v[132:135], v[32:47]\n\t" \
  "v_exp_f32 v51, v51\n\t" \
  "v_exp_f32 v52, v52\n\t" \
  "v_exp_f32 v53, v53\n\t" \
  "v_cvt_pk_bf16_f32 v189, v50, v51\n\t" \
  "v_smfmac_f32_16x16x64_bf16 v[168:171], v[176:179], v[160:167], v180\n\t" \
  "v_exp_f32 v54, v54\n\t" \
  "v_exp_f32 v55, v55\n\t" \
  "v_exp_f32 v56, v56\n\t" \
  "v_cvt_pk_bf16_f32 v190, v52, v53\n\t" \
  "s_waitcnt lgkmcnt(0)\n\t" \
  "v_mfma_f32_32x32x16_bf16 v[64:79], v[156:159], v[164:167], v[64:79]\n\t" \
  "v_exp_f32 v57, v57\n\t" \
  "v_exp_f32 v58, v58\n\t" \
  "v_cvt_pk_bf16_f32 v191, v54, v55\n\t" \
  "v_exp_f32 v59, v59\n\t" \
  "v_exp_f32 v60, v60\n\t" \
  "v_exp_f32 v61, v61\n\t" \
  "v_mfma_f32_32x32x16_bf16 v[80:95], v[152:155], v[188:191], v[80:95]\n\t" \
  "ds_read_b128 v[152:155], v181 offset:14336\n\t" \
  "v_exp_f32 v62, v62\n\t" \
  "v_exp_f32 v63, v63\n\t" \
  "v_cvt_pk_bf16_f32 v192, v56, v57\n\t" \
  "v_cvt_pk_bf16_f32 v193, v58, v59\n\t" \
  "v_cvt_pk_bf16_f32 v194, v60, v61\n\t" \
  "v_cvt_pk_bf16_f32 v195, v62, v63\n\t" \
  "s_waitcnt vmcnt(4)\n\t" \
  "s_barrier\n\t" \
  "v_mfma_f32_32x32x16_bf16 v[48:63], v[144:147], v[136:139], v[112:127]\n\t" \
  "ds_read_b128 v[144:147], v182 offset:0\n\t" \
  "s_mov_b32 m0, s50\n\t" \
  "v_exp_f32 v32, v32\n\t" \
  "v_exp_f32 v33, v33\n\t" \
  "v_exp_f32 v34, v34\n\t" \
  "v_cvt_pk_bf16_f32 v160, v32, v33\n\t" \
  "v_mfma_f32_32x32x16_bf16 v[48:63], v[148:151], v[140:143], v[48:63]\n\t" \
  "ds_read_b128 v[148:151], v182 offset:1024\n\t" \
  "buffer_load_dwordx4 v183, s[44:47], s51 offen offset:0 lds\n\t" \
  "v_exp_f32 v35, v35\n\t" \
  "v_exp_f32 v36, v36\n\t" \
  "v_exp_f32 v37, v37\n\t" \
  "v_cvt_pk_bf16_f32 v161, v34, v35\n\t" \
  "v_smfmac_f32_16x16x64_bf16 v[172:175], v[176:179], v[188:195], v180\n\t" \
  "buffer_load_dwordx4 v183, s[44:47], s51 offen offset:1024 lds\n\t" \
  "v_exp_f32 v38, v38\n\t" \
  "v_exp_f32 v39, v39\n\t" \
  "v_exp_f32 v40, v40\n\t" \
  "v_cvt_pk_bf16_f32 v162, v36, v37\n\t" \
  "v_mfma_f32_32x32x16_bf16 v[80:95], v[156:159], v[192:195], v[80:95]\n\t" \
  "ds_read_b128 v[156:159], v181 offset:15360\n\t" \
  "buffer_load_dwordx4 v183, s[44:47], s51 offen offset:2048 lds\n\t" \
  "v_exp_f32 v41, v41\n\t" \
  "v_exp_f32 v42, v42\n\t" \
  "v_cvt_pk_bf16_f32 v163, v38, v39\n\t" \
  "v_exp_f32 v43, v43\n\t" \
  "v_exp_f32 v44, v44\n\t" \
  "v_exp_f32 v45, v45\n\t" \
  "s_waitcnt lgkmcnt(3)\n\t" \
  "v_mfma_f32_32x32x16_bf16 v[64:79], v[152:155], v[160:163], v[64:79]\n\t" \
  "buffer_load_dwordx4 v183, s[44:47], s51 offen offset:3072 lds\n\t" \
  "v_exp_f32 v46, v46\n\t" \
  "v_exp_f32 v47, v47\n\t" \
  "v_cvt_pk_bf16_f32 v164, v40, v41\n\t" \
  "v_cvt_pk_bf16_f32 v165, v42, v43\n\t" \
  "v_cvt_pk_bf16_f32 v166, v44, v45\n\t" \
  "v_cvt_pk_bf16_f32 v167, v46, v47\n\t" \
  "s_waitcnt lgkmcnt(1)\n\t" \
  "v_mfma_f32_32x32x16_bf16 v[32:47], v[144:147], v[128:131], v[96:111]\n\t" \
  "v_exp_f32 v48, v48\n\t" \
  "v_exp_f32 v49, v49\n\t" \
  "v_exp_f32 v50, v50\n\t" \
  "v_cvt_pk_bf16_f32 v188, v48, v49\n\t" \
  "v_mfma_f32_32x32x16_bf16 v[32:47], v[148:151], v[132:135], v[32:47]\n\t" \
  "v_exp_f32 v51, v51\n\t" \
  "v_exp_f32 v52, v52\n\t" \
  "v_exp_f32 v53, v53\n\t" \
  "v_cvt_pk_bf16_f32 v189, v50, v51\n\t" \
  "v_smfmac_f32_16x16x64_bf16 v[168:171], v[176:179], v[160:167], v180\n\t" \
  "v_exp_f32 v54, v54\n\t" \
  "v_exp_f32 v55, v55\n\t" \
  "v_exp_f32 v56, v56\n\t" \
  "v_cvt_pk_bf16_f32 v190, v52, v53\n\t" \
  "s_waitcnt lgkmcnt(0)\n\t" \
  "v_mfma_f32_32x32x16_bf16 v[64:79], v[156:159], v[164:167], v[64:79]\n\t" \
  "v_exp_f32 v57, v57\n\t" \
  "v_exp_f32 v58, v58\n\t" \
  "v_cvt_pk_bf16_f32 v191, v54, v55\n\t" \
  "v_exp_f32 v59, v59\n\t" \
  "v_exp_f32 v60, v60\n\t" \
  "v_exp_f32 v61, v61\n\t" \
  "v_mfma_f32_32x32x16_bf16 v[80:95], v[152:155], v[188:191], v[80:95]\n\t" \
  "ds_read_b128 v[152:155], v182 offset:2048\n\t" \
  "v_exp_f32 v62, v62\n\t" \
  "v_exp_f32 v63, v63\n\t" \
  "v_cvt_pk_bf16_f32 v192, v56, v57\n\t" \
  "v_cvt_pk_bf16_f32 v193, v58, v59\n\t" \
  "v_cvt_pk_bf16_f32 v194, v60, v61\n\t" \
  "v_cvt_pk_bf16_f32 v195, v62, v63\n\t" \
  "v_mov_b32 v181, v182\n\t" \
  "s_add_u32 s52, s52, 1\n\t" \
  "s_add_u32 s48, s52, 1\n\t" \
  "s_and_b32 s48, s48, 3\n\t" \
  "s_lshl_b32 s48, s48, 15\n\t" \
  "v_add_u32 v182, s48, v184\n\t" \
  "s_add_u32 s51, s51, 0x4000\n\t" \
  "s_add_u32 s48, s52, 3\n\t" \
  "s_and_b32 s48, s48, 3\n\t" \
  "s_lshl_b32 s48, s48, 15\n\t" \
  "s_add_u32 s50, s49, s48\n\t" \
  "s_cmp_lt_u32 s52, 8\n\t" \
  "s_cbranch_scc1 1b\n\t" \
  "; drain: row sums and second P.V half of the last unit\n\t" \
  "v_smfmac_f32_16x16x64_bf16 v[172:175], v[176:179], v[188:195], v180\n\t" \
  "v_mfma_f32_32x32x16_bf16 v[80:95], v[156:159], v[192:195], v[80:95]\n\t" \
  "s_waitcnt vmcnt(0) lgkmcnt(0)\n\t" \
  "s_nop 15\n\t"

constexpr int AT_SLOT = 32768, AT_RED = 4 * AT_SLOT, AT_LDS = AT_RED + 8192;
DI float xhalf_max(float v) { auto r = __builtin_amdgcn_permlane32_swap(__float_as_uint(v), __float_as_uint(v), false, false); return fmaxf(__uint_as_float(r[0]), __uint_as_float(r[1])); }
DI float max16(const f32x16& s) { float a = fmaxf(fmaxf(s[0], s[1]), s[2]);
#pragma unroll
  for (int r = 3; r < 15; r += 2) a = fmaxf(fmaxf(a, s[r]), s[r + 1]);
  return fmaxf(a, s[15]); }
__device__ __noinline__ void attn_slow(const char* kv, const char* qp, char* op, int l) {
  const bf16x8 q0 = *(const bf16x8*)(qp + l * 16), q1 = *(const bf16x8*)(qp + 1024 + l * 16);
  f32x16 o = f32x16{}; float m = -INFINITY, sum = 0.f;
  for (int t = 0; t < 64; ++t) {
    const char* p = kv + t * 4096 + l * 16;
    f32x16 s = MFMA32(*(const bf16x8*)p, q0, f32x16{}); s = MFMA32(*(const bf16x8*)(p + 1024), q1, s);
    const float mn = fmaxf(m, xhalf_max(max16(s))), a = __builtin_amdgcn_exp2f(m - mn); m = mn; float ps = 0.f;
#pragma unroll
    for (int r = 0; r < 16; ++r) { s[r] = rbf(__builtin_amdgcn_exp2f(s[r] - mn)); ps += s[r]; o[r] *= a; }
    sum = sum * a + ps;
    o = MFMA32(*(const bf16x8*)(p + 2048), pack8(s, 0), o); o = MFMA32(*(const bf16x8*)(p + 3072), pack8(s, 1), o);
  }
  auto r = __builtin_amdgcn_permlane32_swap(__float_as_uint(sum), __float_as_uint(sum), false, false);
  o *= 1.f / (__uint_as_float(r[0]) + __uint_as_float(r[1]));
  *(bf16x8*)(op + l * 16) = pack8(o, 0); *(bf16x8*)(op + 1024 + l * 16) = pack8(o, 1);
}
__global__ __launch_bounds__(512, 2) void attn_kernel(char* __restrict__ ws, const u16* woa, const u16* wob STAMP_ARG) {
  extern __shared__ __attribute__((aligned(16))) char smem[];
  LAS char* lds = (LAS char*)smem;
  STAMP(1, 0);
  const int tid = threadIdx.x, l = tid & 63, h = l >> 5;
  const int wv = rfl(tid >> 6), kh = wv >> 2, wq = wv & 3;
  const int head = (blockIdx.x & 7) * 4 + ((blockIdx.x >> 3) & 3), qb = blockIdx.x >> 5;
  const unsigned lds0 = (unsigned)(size_t)lds;
  const __amdgpu_buffer_rsrc_t skv = mk_srd(ws + WS_KV + (size_t)head * 262144, 262144);
  const unsigned vkv = l * 16;
#define AT_DMA(j) do { _Pragma("unroll") for (int p = 0; p < 4; ++p) dma16(skv, vkv, (unsigned)((kh * 32 + 4 * (j) + wq) * 4096 + p * 1024), lds0 + ((j) & 3) * AT_SLOT + wv * 4096 + p * 1024); } while (0)
  AT_DMA(0);
  { LAS f32x4* red = (LAS f32x4*)(lds + AT_RED);
    if (tid < 256) { const char* p = ws + WS_KVP + (size_t)head * 65536 + tid * 16; f32x4 s = *(const f32x4*)p;
#pragma unroll
      for (int r = 1; r < 16; ++r) s += *(const f32x4*)(p + r * 4096);
      red[tid] = s; }
    else if (tid < 264) { const char* p = ws + WS_ZP + (size_t)head * 2048 + (tid - 256) * 16; f32x4 s = *(const f32x4*)p;
#pragma unroll
      for (int r = 1; r < 16; ++r) s += *(const f32x4*)(p + r * 128);
      red[tid] = s; }
  }
  const char* pqp = ws + WS_PQ + (size_t)(head * 64 + qb * 8 + wv) * 2048 + l * 16;
  const bf16x8 aq0 = *(const bf16x8*)pqp, aq1 = *(const bf16x8*)(pqp + 1024);
  bf16x8 qf[2][2];
#pragma unroll
  for (int i = 0; i < 2; ++i) { const char* qp = ws + WS_QB + (size_t)(head * 64 + qb * 8 + wq * 2 + i) * 2048 + l * 16; qf[i][0] = *(const bf16x8*)qp; qf[i][1] = *(const bf16x8*)(qp + 1024); }
  asm volatile("" :: "v"(qf[0][0]), "v"(qf[0][1]), "v"(qf[1][0]), "v"(qf[1][1]), "v"(aq0), "v"(aq1));
  WAIT_BAR(0);
  AT_DMA(1); AT_DMA(2);
  { const int xcd = blockIdx.x & 7; const __amdgpu_buffer_rsrc_t swo = mk_srd((xcd >> 2) ? wob : woa, DM * DM * 2);
    warm4(swo, (unsigned)((blockIdx.x >> 3) * 32 + wv * 4 + ((l >> 3) & 3)) * 2048u + (xcd & 1) * 1024 + (l & 7) * 128, 0u, lds0 + AT_LDS - 256); }
  { const LAS f32x4* red = (const LAS f32x4*)(lds + AT_RED);
    f32x16 kvf, zf, pqf;
#pragma unroll
    for (int e = 0; e < 8; ++e) { pqf[e] = phi((float)aq0[e]); pqf[8 + e] = phi((float)aq1[e]); }
    const bf16x8 pq0 = pack8(pqf, 0), pq1 = pack8(pqf, 1);
#pragma unroll
    for (int g = 0; g < 4; ++g) { const f32x4 a = red[g * 64 + l], b = red[256 + h * 4 + g];
#pragma unroll
      for (int e = 0; e < 4; ++e) { kvf[4 * g + e] = a[e]; zf[4 * g + e] = b[e]; } }
    f32x16 num = MFMA32(pack8(kvf, 0), pq0, f32x16{}); num = MFMA32(pack8(kvf, 1), pq1, num);
    f32x16 den = MFMA32(pack8(zf, 0), pq0, f32x16{}); den = MFMA32(pack8(zf, 1), pq1, den);
    num *= 1.f / (den[0] + 1e-6f);
    char* op = ws + WS_OA + (size_t)((qb * 8 + wv) * 32 + head) * 2048 + l * 16;
    ST16(op, pack8(num, 0)); ST16(op + 1024, pack8(num, 1));
  }
  f32x16 negm[2], o[2]; f32x4 ls[2];
  { const bf16x8 k0 = lds128(lds + l * 16), k1 = lds128(lds + 1024 + l * 16);
#pragma unroll
    for (int i = 0; i < 2; ++i) { f32x16 s = MFMA32(k0, qf[i][0], f32x16{}); s = MFMA32(k1, qf[i][1], s); const float m = -xhalf_max(max16(s));
#pragma unroll
      for (int r = 0; r < 16; ++r) negm[i][r] = m;
      o[i] = f32x16{}; ls[i] = f32x4{}; } }
  u32x4 sel; unsigned sidx;
  { const unsigned v = (l & 3) < 2 ? 0x3f803f80u : 0u, v0 = (l & 4) ? 0u : v, v1 = (l & 4) ? v : 0u; sel = u32x4{v0, v0, v1, v1}; sidx = (l & 1) ? 0xEEEEEEEEu : 0x44444444u; }
  { const unsigned base = lds0 + kh * 16384 + l * 16; unsigned ad = base, adn = base + AT_SLOT;
    const size_t kvh = (size_t)(ws + WS_KV) + (size_t)head * 262144 + kh * 131072;
    const u32x4 srd = {rfl((unsigned)kvh), rfl((unsigned)(kvh >> 32) & 0xffffu), 131072u, 0x00020000u};
    unsigned dst0 = lds0 + wv * 4096, dst = dst0 + 3 * AT_SLOT, soff = (12 + wq) * 4096;
    STAMP(1, 1);
    asm volatile(AT_LOOP_ASM
      : "+{v[64:79]}"(o[0]), "+{v[80:95]}"(o[1]), "+{v[168:171]}"(ls[0]), "+{v[172:175]}"(ls[1]), "+{v181}"(ad), "+{v182}"(adn), "+{s50}"(dst), "+{s51}"(soff)
      : "{v[96:111]}"(negm[0]), "{v[112:127]}"(negm[1]), "{v[128:131]}"(qf[0][0]), "{v[132:135]}"(qf[0][1]), "{v[136:139]}"(qf[1][0]), "{v[140:143]}"(qf[1][1]),
        "{v[176:179]}"(sel), "{v180}"(sidx), "{v183}"(vkv), "{v184}"(base), "{s[44:47]}"(srd), "{s49}"(dst0), "{s54}"(lds0 + 16384)
      : "memory", "scc", "s48", "s52", "v32", "v33", "v34", "v35", "v36", "v37", "v38", "v39", "v40", "v41", "v42", "v43", "v44", "v45", "v46", "v47",
        "v48", "v49", "v50", "v51", "v52", "v53", "v54", "v55", "v56", "v57", "v58", "v59", "v60", "v61", "v62", "v63",
        "v144", "v145", "v146", "v147", "v148", "v149", "v150", "v151", "v152", "v153", "v154", "v155", "v156", "v157", "v158", "v159",
        "v160", "v161", "v162", "v163", "v164", "v165", "v166", "v167", "v188", "v189", "v190", "v191", "v192", "v193", "v194", "v195", "s53");
    STAMP(1, 2);
    __syncthreads();
  }
  { LAS f32x4* ex = (LAS f32x4*)lds;
    const f32x16 og = kh ? o[0] : o[1]; const float lg = kh ? ls[0][0] + ls[0][1] : ls[1][0] + ls[1][1];
#pragma unroll
    for (int g = 0; g < 4; ++g) ex[(wv * 5 + g) * 64 + l] = f32x4{og[4 * g], og[4 * g + 1], og[4 * g + 2], og[4 * g + 3]};
    ((LAS float*)&ex[(wv * 5 + 4) * 64])[l] = lg;
    __syncthreads();
    f32x16 of = kh ? o[1] : o[0]; float lf = kh ? ls[1][0] + ls[1][1] : ls[0][0] + ls[0][1];
    const int pw = wv ^ 4;
#pragma unroll
    for (int g = 0; g < 4; ++g) { const f32x4 a = ex[(pw * 5 + g) * 64 + l];
#pragma unroll
      for (int e = 0; e < 4; ++e) of[4 * g + e] += a[e]; }
    lf += ((const LAS float*)&ex[(pw * 5 + 4) * 64])[l];
    const int tile = qb * 8 + wq * 2 + kh;
    char* op = ws + WS_OB + (size_t)(tile * 32 + head) * 2048;
    float amax = 0.f;
#pragma unroll
    for (int r = 0; r < 16; ++r) amax = fmaxf(amax, fabsf(of[r]));
    const bool ok = lf < 0x1p64f && amax < 0x1p120f;
    if (__all(ok)) { of *= 1.f / lf; ST16(op + l * 16, pack8(of, 0)); ST16(op + 1024 + l * 16, pack8(of, 1)); }
    else attn_slow(ws + WS_KV + (size_t)head * 262144, ws + WS_QB + (size_t)(head * 64 + tile) * 2048, op, l);
  }
  STAMP(1, 3);
#undef AT_DMA
}

constexpr int G3_SLOT = 32768, G3_LDS = 4 * G3_SLOT;
__global__ __launch_bounds__(512, 2) void out_kernel(const u16* woa, const u16* wob, const char* __restrict__ ws, u16* __restrict__ out STAMP_ARG) {
  extern __shared__ __attribute__((aligned(16))) char smem[];
  LAS char* lds = (LAS char*)smem;
  STAMP(2, 0);
  const int tid = threadIdx.x, l = tid & 63, h = l >> 5;
  const int wv = rfl(tid >> 6), w4 = wv & 3, wm = w4 >> 1, wn = w4 & 1;
  const int xcd = blockIdx.x & 7, idx = blockIdx.x >> 3, br = xcd >> 2, rb = ((xcd >> 1) & 1) * 8 + (idx & 7), cb = (xcd & 1) * 4 + (idx >> 3);
  const unsigned lds0 = (unsigned)(size_t)lds;
  if (wv >= 4) {
    const __amdgpu_buffer_rsrc_t so = mk_srd(ws + (br ? WS_OB : WS_OA) + (size_t)rb * 262144, 262144), sw = mk_srd(br ? wob : woa, DM * DM * 2);
    const unsigned vo = l * 16 + w4 * 65536;
    const unsigned vw = (unsigned)(w4 * 16 + (l >> 3)) * 2048u + cb * 256 + ((((l >> 2) & 1) ^ ((l >> 4) & 1)) * 64) + (l & 3) * 16;
#define G3_DMA(kt) do { const unsigned sl = lds0 + ((kt) & 3) * G3_SLOT; \
    _Pragma("unroll") for (int p = 0; p < 4; ++p) { dma16(so, vo, (kt) * 4096u + p * 1024u, sl + w4 * 4096 + p * 1024); \
      dma16(sw, vw, (kt) * (64u * 2048u) + (p & 1) * (8u * 2048u) + (p >> 1) * 128u, sl + 16384 + (p >> 1) * 8192 + w4 * 2048 + (p & 1) * 1024); } } while (0)
    G3_DMA(0); G3_DMA(1); G3_DMA(2);
    WAIT_BAR(16);
    G3_DMA(3);
    for (int kt = 0; kt < 12; ++kt) { WAIT_BAR(16); G3_DMA(kt + 4); }
    WAIT_BAR(16); WAIT_BAR(8); WAIT_BAR(0);
#undef G3_DMA
  } else {
    const LAS char* ao = lds + wm * 8192 + l * 16;
    const LAS char* aw[2];
    { const int q = (l & 15) >> 2, c4 = ((l >> 4) & 1) * 4 + (l & 3), col = 16 * (c4 & 1) + 4 * (c4 >> 1);
#pragma unroll
      for (int n = 0; n < 2; ++n) aw[n] = lds + 16384 + wn * 8192 + (4 * h + q) * 128 + ((n ^ (q >> 1)) * 64) + col * 2; }
    f32x16 acc[2][2];
#pragma unroll
    for (int i = 0; i < 2; ++i)
#pragma unroll
      for (int j = 0; j < 2; ++j) acc[i][j] = f32x16{};
    bf16x8 fo[2][2], fw[2][2];
#define G3_RO(b, so_, s, i) fo[b][i] = lds128(ao + (so_) + (i) * 4096 + (s) * 1024)
#define G3_RW(b, so_, s, i) fw[b][i] = ldstr(aw[i] + (so_) + (s) * 2048, 1024)
#define G3_MM(b, m, n) acc[m][n] = MFMA32(fw[b][n], fo[b][m], acc[m][n])
#define G3_STEP(b, RD, so_, s) do { SBAR(); \
    G3_MM(b, 0, 0); if (RD) { G3_RO(b ^ 1, so_, s, 0); G3_RW(b ^ 1, so_, s, 0); } SBAR(); \
    G3_MM(b, 1, 0); if (RD) { G3_RO(b ^ 1, so_, s, 1); G3_RW(b ^ 1, so_, s, 1); } SBAR(); \
    G3_MM(b, 0, 1); SBAR(); G3_MM(b, 1, 1); SBAR(); } while (0)
    WAIT_BAR(0);
    STAMP(2, 1);
    G3_RO(0, 0, 0, 0); G3_RW(0, 0, 0, 0); G3_RO(0, 0, 0, 1); G3_RW(0, 0, 0, 1);
    for (int kt = 0; kt < 15; ++kt) {
      const int so_ = (kt & 3) * G3_SLOT, sn = ((kt + 1) & 3) * G3_SLOT;
      G3_STEP(0, true, so_, 1); G3_STEP(1, true, so_, 2); G3_STEP(0, true, so_, 3);
      WAIT_BAR(0);
      G3_STEP(1, true, sn, 0);
    }
    G3_STEP(0, true, 3 * G3_SLOT, 1); G3_STEP(1, true, 3 * G3_SLOT, 2); G3_STEP(0, true, 3 * G3_SLOT, 3); G3_STEP(1, false, 0, 0);
    STAMP(2, 2);
#pragma unroll
    for (int m = 0; m < 2; ++m)
#pragma unroll
      for (int n = 0; n < 2; ++n)
#pragma unroll
        for (int s = 0; s < 2; ++s) { const int row = wm * 64 + m * 32 + (l & 31), ch = wn * 8 + n * 4 + h * 2 + s;
          *(LAS bf16x8*)(lds + row * 256 + ((ch ^ (row & 15)) * 16)) = pack8(acc[m][n], s); }
#undef G3_RO
#undef G3_RW
#undef G3_MM
#undef G3_STEP
  }
  __syncthreads();
#pragma unroll
  for (int i = 0; i < 4; ++i) { const int row = wv * 16 + i * 4 + (l >> 4), ch = l & 15;
    *(bf16x8*)(out + (size_t)(rb * 128 + row) * 2048 + br * 1024 + cb * 128 + ch * 8) = lds128(lds + row * 256 + ((ch ^ (row & 15)) * 16)); }
  STAMP(2, 3);
}

extern "C" void kernel_launch(void* const* d_in, const int* in_sizes, int n_in, void* d_out, int out_size, void* d_ws, size_t ws_size, hipStream_t stream) {
  const u16 *xa = (const u16*)d_in[0], *wa = (const u16*)d_in[1], *woa = (const u16*)d_in[2], *xb = (const u16*)d_in[3], *wb = (const u16*)d_in[4], *wob = (const u16*)d_in[5];
  (void)hipFuncSetAttribute((const void*)qkv_kernel, hipFuncAttributeMaxDynamicSharedMemorySize, G1_LDS);
  (void)hipFuncSetAttribute((const void*)attn_kernel, hipFuncAttributeMaxDynamicSharedMemorySize, AT_LDS);
  (void)hipFuncSetAttribute((const void*)out_kernel, hipFuncAttributeMaxDynamicSharedMemorySize, G3_LDS);
  qkv_kernel<<<256, 512, G1_LDS, stream>>>(xa, wa, xb, wb, (char*)d_ws STAMP_PASS);
  attn_kernel<<<256, 512, AT_LDS, stream>>>((char*)d_ws, woa, wob STAMP_PASS);
  out_kernel<<<256, 512, G3_LDS, stream>>>(woa, wob, (const char*)d_ws, (u16*)d_out STAMP_PASS);
}
```

```cpp
#include <hip/hip_runtime.h>
typedef unsigned short u16;
typedef unsigned long long u64;
typedef __bf16 bf16x8 __attribute__((ext_vector_type(8)));
typedef short s16x4 __attribute__((ext_vector_type(4)));
typedef float f32x16 __attribute__((ext_vector_type(16)));
typedef float f32x4 __attribute__((ext_vector_type(4)));
typedef float f32x2 __attribute__((ext_vector_type(2)));
typedef __bf16 bf16x2 __attribute__((ext_vector_type(2)));
typedef unsigned u32x4 __attribute__((ext_vector_type(4)));
#define LAS __attribute__((address_space(3)))
#define GAS __attribute__((address_space(1)))
#define DI __device__ __forceinline__
#define SBAR() __builtin_amdgcn_sched_barrier(0)
#define MFMA32(a, b, c) __builtin_amdgcn_mfma_f32_32x32x16_bf16(a, b, c, 0, 0, 0)
#define MFMA16(a, b, c) __builtin_amdgcn_mfma_f32_16x16x32_bf16(a, b, c, 0, 0, 0)

constexpr int SEQ = 2048, DM = 1024;
constexpr unsigned T2 = 32;
constexpr unsigned WS_QB = 0;
constexpr unsigned WS_KV = WS_QB + (4u << 20);
constexpr unsigned WS_PQ = WS_KV + (8u << 20);
constexpr unsigned WS_OA = WS_PQ + (2u << 20);
constexpr unsigned WS_OB = WS_OA + (4u << 20);
constexpr unsigned WS_KVP = WS_OB + (4u << 20);
constexpr unsigned WS_ZP = WS_KVP + (1u << 20);
constexpr unsigned WS_F1 = WS_ZP + (32u << 10);
constexpr unsigned WS_F2 = WS_F1 + 8192;
constexpr unsigned WS_EP = WS_F2 + 16 * 32 * T2;
constexpr unsigned WS_END = WS_EP + 256;

DI unsigned rfl(unsigned x) { return __builtin_amdgcn_readfirstlane(x); }
DI int lane_id() { int r; asm volatile("v_mbcnt_lo_u32_b32 %0, -1, 0\n\tv_mbcnt_hi_u32_b32 %0, -1, %0" : "=v"(r)); return r; }
DI unsigned cvtpk(float lo, float hi) { f32x2 v = {lo, hi}; bf16x2 b = __builtin_convertvector(v, bf16x2); return __builtin_bit_cast(unsigned, b); }
DI bf16x8 pack8(const f32x16& a, int s) {
  u32x4 w = {cvtpk(a[8 * s], a[8 * s + 1]), cvtpk(a[8 * s + 2], a[8 * s + 3]), cvtpk(a[8 * s + 4], a[8 * s + 5]), cvtpk(a[8 * s + 6], a[8 * s + 7])};
  return __builtin_bit_cast(bf16x8, w);
}
DI bf16x8 pack44(const f32x4& a, const f32x4& b) { u32x4 w = {cvtpk(a[0], a[1]), cvtpk(a[2], a[3]), cvtpk(b[0], b[1]), cvtpk(b[2], b[3])}; return __builtin_bit_cast(bf16x8, w); }
DI float rbf(float x) { return (float)(__bf16)x; }
DI f32x4 phi4(const f32x4& t) {
  const f32x4 u = t * 1.4426950408889634f; f32x4 e, m;
#pragma unroll
  for (int r = 0; r < 4; ++r) { e[r] = __builtin_amdgcn_fmed3f(__builtin_amdgcn_exp2f(u[r]), 0.f, 1.f); m[r] = __builtin_amdgcn_fmed3f(t[r], 1e-6f, 3.0e38f); }
  return m + e;
}
typedef __amdgpu_buffer_rsrc_t srd_t;
DI srd_t mk_srd(const void* p, unsigned bytes) { return __builtin_amdgcn_make_buffer_rsrc((void*)p, (short)0, (int)bytes, 0x00020000); }
DI void dma16(srd_t srd, unsigned voff, unsigned soff, unsigned ldsdst) {
  asm volatile("s_mov_b32 m0, %0\n\ts_nop 0\n\tbuffer_load_dwordx4 %1, %2, %3 offen lds" :: "s"(ldsdst), "v"(voff), "s"(srd), "s"(soff) : "memory");
}
DI void dma16c(srd_t srd, unsigned voff, unsigned soff, unsigned ldsdst) {
  asm volatile("s_mov_b32 m0, %0\n\ts_nop 0\n\tbuffer_load_dwordx4 %1, %2, %3 offen sc1 lds" :: "s"(ldsdst), "v"(voff), "s"(srd), "s"(soff) : "memory");
}
DI void warm4(srd_t srd, unsigned voff, unsigned soff, unsigned ldsdst) {
  asm volatile("s_mov_b32 m0, %0\n\ts_nop 0\n\tbuffer_load_dword %1, %2, %3 offen lds" :: "s"(ldsdst), "v"(voff), "s"(srd), "s"(soff) : "memory");
}
DI u32x4 code_srd() {
  unsigned lo, hi, len;
  asm volatile("s_getpc_b64 s[60:61]\n\ts_add_u32 %0, s60, fused_kernel@rel32@lo+4\n\ts_addc_u32 %1, s61, fused_kernel@rel32@hi+12\n\ts_mov_b32 %2, code_end_mark-fused_kernel"
               : "=s"(lo), "=s"(hi), "=s"(len) :: "s60", "s61", "scc");
  return u32x4{lo, hi & 0xffffu, len, 0x00020000u};
}
#define WARM_CODE(n, THEN) do { unsigned d0, d1, d2, d3; \
  asm volatile("s_buffer_load_dword %0, %4, %5\n\ts_buffer_load_dword %1, %4, %5 offset:64\n\ts_buffer_load_dword %2, %4, %5 offset:128\n\ts_buffer_load_dword %3, %4, %5 offset:192\n\t" \
               THEN "s_waitcnt lgkmcnt(0)" : "=&s"(d0), "=&s"(d1), "=&s"(d2), "=&s"(d3) : "s"(code_srd()), "s"((n) * 256) : "memory"); } while (0)
template <class T> DI void st16c(srd_t ws, unsigned voff, unsigned soff, T v) {
  asm volatile("buffer_store_dwordx4 %0, %1, %2, %3 offen sc1\n\ts_nop 1" :: "v"(v), "v"(voff), "s"(ws), "s"(soff) : "memory");
}
DI void st_out(u16* p, u32x4 v) { asm volatile("global_store_dwordx4 %0, %1, off sc0 sc1\n\ts_nop 1" :: "v"(p), "v"(v) : "memory"); }
DI u32x4 ld16c(srd_t ws, unsigned voff, unsigned soff) { return __builtin_amdgcn_raw_buffer_load_b128(ws, voff, soff, 16); }
#define BAR() asm volatile("s_barrier" ::: "memory")
#define WAIT_BAR(N) asm volatile("s_waitcnt vmcnt(" #N ") lgkmcnt(0)\n\ts_barrier" ::: "memory")
#define WAIT_LBAR() asm volatile("s_waitcnt lgkmcnt(0)\n\ts_barrier" ::: "memory")
DI bf16x8 lds128(const LAS char* p) { return *(const LAS bf16x8*)p; }
DI void put_tag(char* ws, unsigned off, u64 tag) { __hip_atomic_store((GAS u64*)(ws + off), tag, __ATOMIC_RELAXED, __HIP_MEMORY_SCOPE_AGENT); }
DI void poll_tags(const GAS u64* p, u64 tag) {
  const u64 t0 = __builtin_amdgcn_s_memrealtime();
  while (!__all(__hip_atomic_load(p, __ATOMIC_RELAXED, __HIP_MEMORY_SCOPE_AGENT) == tag)) {
    if (__builtin_amdgcn_s_memrealtime() - t0 > 2000000ull) break;
    __builtin_amdgcn_s_sleep(2);
  }
}
DI void wait_tags(char* ws, unsigned off, int n, int stride, u64 tag, int w4, int l, volatile LAS unsigned* word, unsigned seq) {
  if (w4 == 0) { poll_tags((const GAS u64*)(ws + off) + (l & (n - 1)) * stride, tag); *word = seq; }
  else { const u64 t0 = __builtin_amdgcn_s_memrealtime();
    while (rfl(*word) < seq) { if (__builtin_amdgcn_s_memrealtime() - t0 > 4000000ull) break; __builtin_amdgcn_s_sleep(1); } }
  asm volatile("" ::: "memory");
}

constexpr int G1_XS = 32768, G1_WS = 12288, G1_WOFF = 3 * G1_XS, LDS_PQ = G1_WOFF + 2 * G1_WS  , LDS_DUMMY = G1_WOFF + 5 * G1_WS, LDS_WORD = LDS_DUMMY + 1024, LDS_BYTES = LDS_WORD + 16;
constexpr int LDS_KVP = 2 * G1_XS, LDS_ZP = G1_WOFF + G1_WS + 8192;
struct QkvAddr { unsigned ax[2], aw[2], vh; bool help; srd_t sw; };
DI QkvAddr qkv_setup(const u16* w, int rb, int cb, int wv, int l, unsigned lds0) {
  QkvAddr q; const int wm = wv >> 1, wn = wv & 1, c = l & 15, g = l >> 4;
#pragma unroll
  for (int u = 0; u < 2; ++u) {
    q.ax[u] = lds0 + (wm * 64 + c) * 128 + (((4 * u + g) ^ ((c >> 1) & 7)) * 16);
    q.aw[u] = lds0 + G1_WOFF + (8 * g + (c >> 2)) * 128 + (((2 * wn + u) ^ (((c >> 3) & 1) | ((g & 1) << 1))) * 32) + (c & 3) * 8;
  }
  const int hot = ((3 - (int)((size_t)w >> 7) - (cb & ~3)) & 7);
  const unsigned hid = (((cb & 3) - ((cb & 3) > hot)) * 4 + (rb & 3)) * 64 + l;
  q.help = wv == 1 && hot < 4 && hot != (cb & 3); q.vh = (hid / 3) * 6144u + (hid % 3) * 2048u + ((cb & ~3) + hot) * 128; q.sw = mk_srd(w, DM * 3 * DM * 2);
  asm volatile("" : "+v"(q.ax[0]), "+v"(q.ax[1]), "+v"(q.aw[0]), "+v"(q.aw[1]), "+v"(q.vh));
  return q;
}
template <bool BR_A>
DI void qkv_compute(char* ws, srd_t sws, const QkvAddr& q, int rb, int cb, int wv, LAS char* lds) {
  const int wm = wv >> 1, wn = wv & 1;
  const LAS char* const ax[2] = {(const LAS char*)(size_t)q.ax[0], (const LAS char*)(size_t)q.ax[1]}; const LAS char* const aw[2] = {(const LAS char*)(size_t)q.aw[0], (const LAS char*)(size_t)q.aw[1]};
  f32x4 acc[4][6]; bf16x8 fx[2][4]; s16x4 fw[6][2];
#define G1_RX(b, xo, u, i) fx[b][i] = lds128(ax[u] + (xo) + (i) * 2048)
#define G1_RW(wo, j, t) fw[j][t] = __builtin_amdgcn_ds_read_tr16_b64_v4i16((LAS s16x4*)(aw[(j) & 1] + (wo) + ((j) >> 1) * 4096 + (t) * 512))
#define G1_FW(j) __builtin_bit_cast(bf16x8, __builtin_shufflevector(fw[j][0], fw[j][1], 0, 1, 2, 3, 4, 5, 6, 7))
#define G1_MM1(Z, b, i, j) acc[i][j] = ((j) >= 4 || (BR_A && (j) >= 2)) ? MFMA16(fx[b][i], wf, (Z) ? f32x4{} : acc[i][j]) : MFMA16(wf, fx[b][i], (Z) ? f32x4{} : acc[i][j])
#define G1_MM(Z, b, i, j) G1_MM1(Z, b, ((j) & 1) ? 3 - (i) : (i), j)
#define G1_GRP(Z, b, j, R0, R1, R2) do { const bf16x8 wf = G1_FW(j); \
    G1_MM(Z, b, 0, j); SBAR(); R0; SBAR(); G1_MM(Z, b, 1, j); SBAR(); R1; SBAR(); G1_MM(Z, b, 2, j); SBAR(); R2; SBAR(); G1_MM(Z, b, 3, j); SBAR(); } while (0)
#define G1_STEP(Z, b, RD, wc, xo, u, wo) G1_STEPB(Z, b, BAR(), RD, wc, xo, u, wo)
#define G1_STEPB(Z, b, BARRIER, RD, wc, xo, u, wo) do { SBAR(); \
    G1_GRP(Z, b, 0, G1_RW(wc, 5, 0), G1_RW(wc, 5, 1), ); BARRIER; SBAR(); \
    G1_GRP(Z, b, 1, if (RD) G1_RW(wo, 0, 0), if (RD) G1_RW(wo, 0, 1), if (RD) G1_RX(b ^ 1, xo, u, 0)); \
    G1_GRP(Z, b, 2, if (RD) G1_RW(wo, 1, 0), if (RD) G1_RW(wo, 1, 1), if (RD) G1_RX(b ^ 1, xo, u, 1)); \
    G1_GRP(Z, b, 3, if (RD) G1_RW(wo, 2, 0), if (RD) G1_RW(wo, 2, 1), if (RD) G1_RX(b ^ 1, xo, u, 2)); \
    G1_GRP(Z, b, 4, if (RD) G1_RW(wo, 3, 0), if (RD) G1_RW(wo, 3, 1), if (RD) G1_RX(b ^ 1, xo, u, 3)); \
    G1_GRP(Z, b, 5, if (RD) G1_RW(wo, 4, 0), if (RD) G1_RW(wo, 4, 1), ); } while (0)
#define G1_NW(w) ((w) == 4 * G1_WS ? 0 : (w) + G1_WS)
  if (q.help) warm4(q.sw, q.vh, 2 * (64u * 6144u), (unsigned)(size_t)lds + LDS_DUMMY);
  G1_RW(0, 0, 0); G1_RW(0, 0, 1); G1_RX(0, 0, 0, 0); G1_RX(0, 0, 0, 1); G1_RX(0, 0, 0, 2); G1_RX(0, 0, 0, 3);
#pragma unroll
  for (int j = 1; j < 5; ++j) { G1_RW(0, j, 0); G1_RW(0, j, 1); }
  G1_STEP(true, 0, true, 0, 0, 1, G1_WS);
  int xo = G1_XS, w0 = G1_WS;
  for (int t = 1; t < (BR_A ? 16 : 15); ++t) {
    const int w1 = G1_NW(w0), w2 = G1_NW(w1);
    if (q.help && (t & 3) == 0) warm4(q.sw, q.vh, (t + 2) * (64u * 6144u), (unsigned)(size_t)lds + LDS_DUMMY);
    G1_STEP(false, 1, true, w0, xo, 0, w1);
    G1_STEP(false, 0, true, w1, xo, 1, w2);
    xo = xo == 2 * G1_XS ? 0 : xo + G1_XS; w0 = w2;
  }
  const unsigned t0 = (2 * cb + wn) * 64 + rb * 8 + wm * 2;
  if constexpr (BR_A) G1_STEPB(false, 1, , false, w0, 0, 0, 0);
  else {
    G1_STEP(false, 1, true, 4 * G1_WS, 0, 0, 0);
#define G1_SK(m, u) st16c(sws, vk + (u) * 128, WS_KV + (t0 + (m)) * 4096, pack44(acc[2 * (m) + (u)][2], acc[2 * (m) + (u)][3]))
#define G1_SV(m, u) st16c(sws, vv + (u) * 256 + 2048, WS_KV + (t0 + (m)) * 4096, pack44(acc[2 * (m)][4 + (u)], acc[2 * (m) + 1][4 + (u)]))
    SBAR();
    G1_GRP(false, 0, 2, G1_RX(1, 0, 1, 0), G1_RX(1, 0, 1, 1), G1_RX(1, 0, 1, 2));
    G1_GRP(false, 0, 3, G1_RW(G1_WS, 2, 0), G1_RW(G1_WS, 2, 1), G1_RX(1, 0, 1, 3));
    G1_GRP(false, 0, 4, G1_RW(G1_WS, 3, 0), G1_RW(G1_WS, 3, 1), G1_RW(0, 5, 0));
    G1_GRP(false, 1, 2, G1_RW(0, 5, 1), G1_RW(G1_WS, 4, 0), G1_RW(G1_WS, 4, 1));
    G1_GRP(false, 1, 3, , , );
    const unsigned l2 = lane_id(), vv = (l2 >> 4) * 512 + (l2 & 15) * 16, vk = vv + (l2 & 8) * 16;
    G1_GRP(false, 0, 5, , , );
    G1_GRP(false, 1, 4, G1_RW(G1_WS, 5, 0); G1_SK(0, 0), G1_RW(G1_WS, 5, 1), G1_SK(0, 1));
    G1_GRP(false, 0, 0, G1_SK(1, 0), , G1_SK(1, 1));
    G1_GRP(false, 1, 5, G1_RW(G1_WS, 0, 0), G1_RW(G1_WS, 0, 1), );
    G1_GRP(false, 0, 1, G1_SV(0, 0), , G1_SV(1, 0));
    G1_GRP(false, 1, 0, G1_RW(G1_WS, 1, 0); G1_RW(G1_WS, 1, 1), G1_SV(0, 1), G1_SV(1, 1));
    G1_GRP(false, 1, 1, , , );
#undef G1_SK
#undef G1_SV
  }
#undef G1_RX
#undef G1_RW
#undef G1_FW
#undef G1_MM
#undef G1_MM1
#undef G1_GRP
#undef G1_STEP
#undef G1_STEPB
  if constexpr (BR_A) WAIT_LBAR();
  const int l = lane_id(), ce = l & 15, ge = l >> 4; const unsigned chunk = ge * 512;
  if constexpr (!BR_A) {
    constexpr float QS = 0.17677669529663687f * 1.4426950408889634f;
#pragma unroll
    for (int i = 0; i < 4; ++i) st16c(sws, chunk + ce * 16 + (i & 1) * 256, WS_QB + (t0 + (i >> 1)) * 2048, pack44(acc[i][0] * QS, acc[i][1] * QS));
    WAIT_LBAR();
    asm volatile("s_waitcnt vmcnt(0)" ::: "memory");
    if (l == 0 && __hip_atomic_fetch_add((LAS unsigned*)(lds + LDS_WORD + 4), 1u, __ATOMIC_RELAXED, __HIP_MEMORY_SCOPE_WORKGROUP) == 7)
      put_tag(ws, WS_F1 + (16 + cb) * 256 + rb * 8, *(volatile LAS u64*)(lds + LDS_WORD + 8));
  } else {
    f32x4 kvp[2][2] = {}, zp[2] = {};
    const u32x4 onesw = {0x3f803f80u, 0x3f803f80u, 0x3f803f80u, 0x3f803f80u}; const bf16x8 ones = __builtin_bit_cast(bf16x8, onesw);
#pragma unroll
    for (int m = 0; m < 2; ++m) {
      const bf16x8 v0 = pack44(acc[2 * m][4], acc[2 * m + 1][4]), v1 = pack44(acc[2 * m][5], acc[2 * m + 1][5]);
#pragma unroll
      for (int a = 0; a < 2; ++a) {
        const bf16x8 pf = pack44(phi4(acc[2 * m][2 + a]), phi4(acc[2 * m + 1][2 + a]));
        kvp[0][a] = MFMA16(pf, v0, kvp[0][a]); kvp[1][a] = MFMA16(pf, v1, kvp[1][a]); zp[a] = MFMA16(pf, ones, zp[a]); }
    }
#pragma unroll
    for (int i = 0; i < 4; ++i) *(LAS bf16x8*)(lds + LDS_PQ + (wn * 8 + wm * 2 + (i >> 1)) * 2048 + chunk + ce * 16 + (i & 1) * 256) = pack44(phi4(acc[i][0]), phi4(acc[i][1]));
#pragma unroll
    for (int i = 0; i < 4; ++i) ((LAS f32x4*)(lds + LDS_KVP))[(wv * 4 + i) * 64 + l] = kvp[i >> 1][i & 1];
    if (ce == 0) { ((LAS f32x4*)(lds + LDS_ZP))[wv * 8 + ge] = zp[0]; ((LAS f32x4*)(lds + LDS_ZP))[wv * 8 + 4 + ge] = zp[1]; }
  }
}
DI u64 qkv_stage(char* ws, const u16* xa, const u16* wa, const u16* xb, const u16* wb, bool bra, int rb, int cb, int w4, int l, unsigned lds0) {
  unsigned vx[2];
#pragma unroll
  for (int i = 0; i < 2; ++i) vx[i] = (unsigned)(rb * 256 + w4 * 32 + (l >> 3)) * 2048u + (unsigned)(((l & 7) ^ ((4 * i + (l >> 4)) & 7)) * 16);
  unsigned vw = (unsigned)(w4 * 8 + (l >> 3)) * 6144u + cb * 128 + ((((l >> 1) & 3) ^ (((l >> 4) & 1) | ((w4 & 1) << 1))) * 32) + (l & 1) * 16;
  unsigned xs = lds0 + w4 * 4096, wd = lds0 + G1_WOFF + 4 * G1_WS + w4 * 1024;
  asm volatile("" : "+v"(vx[0]), "+v"(vx[1]), "+v"(vw)); SBAR();
  const srd_t sx = mk_srd(bra ? xa : xb, SEQ * DM * 2), sw = mk_srd(bra ? wa : wb, DM * 3 * DM * 2);
#pragma nounroll
  for (int b = -5; b < 27; ++b) {
    if (b >= -1) asm volatile("s_waitcnt vmcnt(14)\n\ts_barrier" ::: "memory");
    const int hw = b + 4, u = b + 5;
    const unsigned sow = hw >= 0 ? hw * (32u * 6144u) : 0x80000000u, sox = (u >> 1) * 128u + (u & 1) * (128u * 2048u);
#pragma unroll
    for (int p = 0; p < 4; ++p) { if (p < 3) dma16(sw, vw, sow + p * 2048u, wd + p * 4096); dma16(sx, vx[p & 1], sox + p * 16384u, xs + p * 1024); }
    xs += 16384; if (xs >= lds0 + G1_WOFF) xs -= G1_WOFF;
    wd += G1_WS; if (wd >= lds0 + LDS_DUMMY) wd -= 5 * G1_WS;
  }
  *(volatile LAS unsigned*)(size_t)(lds0 + LDS_WORD + 4) = 0;
  *(volatile LAS unsigned*)(size_t)(lds0 + LDS_WORD) = 0;
  asm volatile("s_waitcnt vmcnt(14)\n\ts_barrier" ::: "memory");
#pragma unroll
  for (int p = 0; p < 3; ++p) dma16(sw, vw, 31 * (32u * 6144u) + p * 2048u, wd + p * 4096);
  const u64 prev = __hip_atomic_load((const GAS u64*)(ws + WS_EP), __ATOMIC_RELAXED, __HIP_MEMORY_SCOPE_AGENT);
  WARM_CODE(((blockIdx.x >> 3) ^ 16) * 4 + w4, "s_waitcnt vmcnt(10)\n\ts_barrier\n\ts_waitcnt vmcnt(0)\n\ts_barrier\n\t");
  if (bra) BAR();
  return prev + 1;
}

#define AT_LOOP_ASM \
  "v_mov_b32 v165, v167\n\t" \
  "v_add_u32 v166, 0x8000, v167\n\t" \
  "ds_read_b128 v[120:123], v165 offset:0\n\t" \
  "ds_read_b128 v[124:127], v165 offset:1024\n\t" \
  "ds_read_b128 v[128:131], v165 offset:2048\n\t" \
  "s_mov_b32 s52, 0\n\t" \
  "s_mov_b32 s48, 0x8000\n\t" \
  "s_waitcnt lgkmcnt(1)\n\t" \
  "v_mfma_f32_32x32x16_bf16 v[8:23], v[120:123], v[104:107], v[72:87]\n\t" \
  "v_mfma_f32_32x32x16_bf16 v[8:23], v[124:127], v[108:111], v[8:23]\n\t" \
  "s_nop 11\n\t" \
  "1:\n\t" \
  "v_mfma_f32_32x32x16_bf16 v[24:39], v[120:123], v[112:115], v[88:103]\n\t" \
  "ds_read_b128 v[120:123], v165 offset:4096\n\t" \
  "v_exp_f32 v8, v8\n\t" \
  "v_exp_f32 v9, v9\n\t" \
  "v_exp_f32 v10, v10\n\t" \
  "v_cvt_pk_bf16_f32 v136, v8, v9\n\t" \
  "v_mfma_f32_32x32x16_bf16 v[24:39], v[124:127], v[116:119], v[24:39]\n\t" \
  "ds_read_b128 v[124:127], v165 offset:5120\n\t" \
  "v_exp_f32 v11, v11\n\t" \
  "v_exp_f32 v12, v12\n\t" \
  "v_exp_f32 v13, v13\n\t" \
  "v_cvt_pk_bf16_f32 v137, v10, v11\n\t" \
  "v_smfmac_f32_16x16x64_bf16 v[156:159], v[160:163], v[144:151], v164\n\t" \
  "v_exp_f32 v14, v14\n\t" \
  "v_exp_f32 v15, v15\n\t" \
  "v_exp_f32 v16, v16\n\t" \
  "v_cvt_pk_bf16_f32 v138, v12, v13\n\t" \
  "v_mfma_f32_32x32x16_bf16 v[56:71], v[132:135], v[148:151], v[56:71]\n\t" \
  "ds_read_b128 v[132:135], v165 offset:3072\n\t" \
  "v_exp_f32 v17, v17\n\t" \
  "v_exp_f32 v18, v18\n\t" \
  "v_cvt_pk_bf16_f32 v139, v14, v15\n\t" \
  "v_exp_f32 v19, v19\n\t" \
  "v_exp_f32 v20, v20\n\t" \
  "v_exp_f32 v21, v21\n\t" \
  "s_waitcnt lgkmcnt(3)\n\t" \
  "v_mfma_f32_32x32x16_bf16 v[40:55], v[128:131], v[136:139], v[40:55]\n\t" \
  "v_exp_f32 v22, v22\n\t" \
  "v_exp_f32 v23, v23\n\t" \
  "v_cvt_pk_bf16_f32 v140, v16, v17\n\t" \
  "v_cvt_pk_bf16_f32 v141, v18, v19\n\t" \
  "v_cvt_pk_bf16_f32 v142, v20, v21\n\t" \
  "v_cvt_pk_bf16_f32 v143, v22, v23\n\t" \
  "s_waitcnt lgkmcnt(1)\n\t" \
  "v_mfma_f32_32x32x16_bf16 v[8:23], v[120:123], v[104:107], v[72:87]\n\t" \
  "v_exp_f32 v24, v24\n\t" \
  "v_exp_f32 v25, v25\n\t" \
  "v_exp_f32 v26, v26\n\t" \
  "v_cvt_pk_bf16_f32 v144, v24, v25\n\t" \
  "v_mfma_f32_32x32x16_bf16 v[8:23], v[124:127], v[108:111], v[8:23]\n\t" \
  "v_exp_f32 v27, v27\n\t" \
  "v_exp_f32 v28, v28\n\t" \
  "v_exp_f32 v29, v29\n\t" \
  "v_cvt_pk_bf16_f32 v145, v26, v27\n\t" \
  "v_smfmac_f32_16x16x64_bf16 v[152:155], v[160:163], v[136:143], v164\n\t" \
  "v_exp_f32 v30, v30\n\t" \
  "v_exp_f32 v31, v31\n\t" \
  "v_exp_f32 v32, v32\n\t" \
  "v_cvt_pk_bf16_f32 v146, v28, v29\n\t" \
  "s_waitcnt lgkmcnt(0)\n\t" \
  "v_mfma_f32_32x32x16_bf16 v[40:55], v[132:135], v[140:143], v[40:55]\n\t" \
  "v_exp_f32 v33, v33\n\t" \
  "v_exp_f32 v34, v34\n\t" \
  "v_cvt_pk_bf16_f32 v147, v30, v31\n\t" \
  "v_exp_f32 v35, v35\n\t" \
  "v_exp_f32 v36, v36\n\t" \
  "v_exp_f32 v37, v37\n\t" \
  "v_mfma_f32_32x32x16_bf16 v[56:71], v[128:131], v[144:147], v[56:71]\n\t" \
  "ds_read_b128 v[128:131], v165 offset:6144\n\t" \
  "v_exp_f32 v38, v38\n\t" \
  "v_exp_f32 v39, v39\n\t" \
  "v_cvt_pk_bf16_f32 v148, v32, v33\n\t" \
  "v_cvt_pk_bf16_f32 v149, v34, v35\n\t" \
  "v_cvt_pk_bf16_f32 v150, v36, v37\n\t" \
  "v_cvt_pk_bf16_f32 v151, v38, v39\n\t" \
  "v_mfma_f32_32x32x16_bf16 v[24:39], v[120:123], v[112:115], v[88:103]\n\t" \
  "ds_read_b128 v[120:123], v165 offset:8192\n\t" \
  "v_exp_f32 v8, v8\n\t" \
  "v_exp_f32 v9, v9\n\t" \
  "v_exp_f32 v10, v10\n\t" \
  "v_cvt_pk_bf16_f32 v136, v8, v9\n\t" \
  "v_mfma_f32_32x32x16_bf16 v[24:39], v[124:127], v[116:119], v[24:39]\n\t" \
  "ds_read_b128 v[124:127], v165 offset:9216\n\t" \
  "v_exp_f32 v11, v11\n\t" \
  "v_exp_f32 v12, v12\n\t" \
  "v_exp_f32 v13, v13\n\t" \
  "v_cvt_pk_bf16_f32 v137, v10, v11\n\t" \
  "v_smfmac_f32_16x16x64_bf16 v[156:159], v[160:163], v[144:151], v164\n\t" \
  "v_exp_f32 v14, v14\n\t" \
  "v_exp_f32 v15, v15\n\t" \
  "v_exp_f32 v16, v16\n\t" \
  "v_cvt_pk_bf16_f32 v138, v12, v13\n\t" \
  "v_mfma_f32_32x32x16_bf16 v[56:71], v[132:135], v[148:151], v[56:71]\n\t" \
  "ds_read_b128 v[132:135], v165 offset:7168\n\t" \
  "v_exp_f32 v17, v17\n\t" \
  "v_exp_f32 v18, v18\n\t" \
  "v_cvt_pk_bf16_f32 v139, v14, v15\n\t" \
  "v_exp_f32 v19, v19\n\t" \
  "v_exp_f32 v20, v20\n\t" \
  "v_exp_f32 v21, v21\n\t" \
  "s_waitcnt lgkmcnt(3)\n\t" \
  "v_mfma_f32_32x32x16_bf16 v[40:55], v[128:131], v[136:139], v[40:55]\n\t" \
  "v_exp_f32 v22, v22\n\t" \
  "v_exp_f32 v23, v23\n\t" \
  "v_cvt_pk_bf16_f32 v140, v16, v17\n\t" \
  "v_cvt_pk_bf16_f32 v141, v18, v19\n\t" \
  "v_cvt_pk_bf16_f32 v142, v20, v21\n\t" \
  "v_cvt_pk_bf16_f32 v143, v22, v23\n\t" \
  "s_waitcnt lgkmcnt(1)\n\t" \
  "v_mfma_f32_32x32x16_bf16 v[8:23], v[120:123], v[104:107], v[72:87]\n\t" \
  "v_exp_f32 v24, v24\n\t" \
  "v_exp_f32 v25, v25\n\t" \
  "v_exp_f32 v26, v26\n\t" \
  "v_cvt_pk_bf16_f32 v144, v24, v25\n\t" \
  "v_mfma_f32_32x32x16_bf16 v[8:23], v[124:127], v[108:111], v[8:23]\n\t" \
  "v_exp_f32 v27, v27\n\t" \
  "v_exp_f32 v28, v28\n\t" \
  "v_exp_f32 v29, v29\n\t" \
  "v_cvt_pk_bf16_f32 v145, v26, v27\n\t" \
  "v_smfmac_f32_16x16x64_bf16 v[152:155], v[160:163], v[136:143], v164\n\t" \
  "v_exp_f32 v30, v30\n\t" \
  "v_exp_f32 v31, v31\n\t" \
  "v_exp_f32 v32, v32\n\t" \
  "v_cvt_pk_bf16_f32 v146, v28, v29\n\t" \
  "s_waitcnt lgkmcnt(0)\n\t" \
  "v_mfma_f32_32x32x16_bf16 v[40:55], v[132:135], v[140:143], v[40:55]\n\t" \
  "v_exp_f32 v33, v33\n\t" \
  "v_exp_f32 v34, v34\n\t" \
  "v_cvt_pk_bf16_f32 v147, v30, v31\n\t" \
  "v_exp_f32 v35, v35\n\t" \
  "v_exp_f32 v36, v36\n\t" \
  "v_exp_f32 v37, v37\n\t" \
  "v_mfma_f32_32x32x16_bf16 v[56:71], v[128:131], v[144:147], v[56:71]\n\t" \
  "ds_read_b128 v[128:131], v165 offset:10240\n\t" \
  "v_exp_f32 v38, v38\n\t" \
  "v_exp_f32 v39, v39\n\t" \
  "v_cvt_pk_bf16_f32 v148, v32, v33\n\t" \
  "v_cvt_pk_bf16_f32 v149, v34, v35\n\t" \
  "v_cvt_pk_bf16_f32 v150, v36, v37\n\t" \
  "v_cvt_pk_bf16_f32 v151, v38, v39\n\t" \
  "v_mfma_f32_32x32x16_bf16 v[24:39], v[120:123], v[112:115], v[88:103]\n\t" \
  "ds_read_b128 v[120:123], v165 offset:12288\n\t" \
  "v_exp_f32 v8, v8\n\t" \
  "v_exp_f32 v9, v9\n\t" \
  "v_exp_f32 v10, v10\n\t" \
  "v_cvt_pk_bf16_f32 v136, v8, v9\n\t" \
  "v_mfma_f32_32x32x16_bf16 v[24:39], v[124:127], v[116:119], v[24:39]\n\t" \
  "ds_read_b128 v[124:127], v165 offset:13312\n\t" \
  "v_exp_f32 v11, v11\n\t" \
  "v_exp_f32 v12, v12\n\t" \
  "v_exp_f32 v13, v13\n\t" \
  "v_cvt_pk_bf16_f32 v137, v10, v11\n\t" \
  "v_smfmac_f32_16x16x64_bf16 v[156:159], v[160:163], v[144:151], v164\n\t" \
  "v_exp_f32 v14, v14\n\t" \
  "v_exp_f32 v15, v15\n\t" \
  "v_exp_f32 v16, v16\n\t" \
  "v_cvt_pk_bf16_f32 v138, v12, v13\n\t" \
  "v_mfma_f32_32x32x16_bf16 v[56:71], v[132:135], v[148:151], v[56:71]\n\t" \
  "ds_read_b128 v[132:135], v165 offset:11264\n\t" \
  "v_exp_f32 v17, v17\n\t" \
  "v_exp_f32 v18, v18\n\t" \
  "v_cvt_pk_bf16_f32 v139, v14, v15\n\t" \
  "v_exp_f32 v19, v19\n\t" \
  "v_exp_f32 v20, v20\n\t" \
  "v_exp_f32 v21, v21\n\t" \
  "s_waitcnt lgkmcnt(3)\n\t" \
  "v_mfma_f32_32x32x16_bf16 v[40:55], v[128:131], v[136:139], v[40:55]\n\t" \
  "v_exp_f32 v22, v22\n\t" \
  "v_exp_f32 v23, v23\n\t" \
  "v_cvt_pk_bf16_f32 v140, v16, v17\n\t" \
  "v_cvt_pk_bf16_f32 v141, v18, v19\n\t" \
  "v_cvt_pk_bf16_f32 v142, v20, v21\n\t" \
  "v_cvt_pk_bf16_f32 v143, v22, v23\n\t" \
  "s_waitcnt lgkmcnt(1)\n\t" \
  "v_mfma_f32_32x32x16_bf16 v[8:23], v[120:123], v[104:107], v[72:87]\n\t" \
  "v_exp_f32 v24, v24\n\t" \
  "v_exp_f32 v25, v25\n\t" \
  "v_exp_f32 v26, v26\n\t" \
  "v_cvt_pk_bf16_f32 v144, v24, v25\n\t" \
  "v_mfma_f32_32x32x16_bf16 v[8:23], v[124:127], v[108:111], v[8:23]\n\t" \
  "v_exp_f32 v27, v27\n\t" \
  "v_exp_f32 v28, v28\n\t" \
  "v_exp_f32 v29, v29\n\t" \
  "v_cvt_pk_bf16_f32 v145, v26, v27\n\t" \
  "v_smfmac_f32_16x16x64_bf16 v[152:155], v[160:163], v[136:143], v164\n\t" \
  "v_exp_f32 v30, v30\n\t" \
  "v_exp_f32 v31, v31\n\t" \
  "v_exp_f32 v32, v32\n\t" \
  "v_cvt_pk_bf16_f32 v146, v28, v29\n\t" \
  "s_waitcnt lgkmcnt(0)\n\t" \
  "v_mfma_f32_32x32x16_bf16 v[40:55], v[132:135], v[140:143], v[40:55]\n\t" \
  "v_exp_f32 v33, v33\n\t" \
  "v_exp_f32 v34, v34\n\t" \
  "v_cvt_pk_bf16_f32 v147, v30, v31\n\t" \
  "v_exp_f32 v35, v35\n\t" \
  "v_exp_f32 v36, v36\n\t" \
  "v_exp_f32 v37, v37\n\t" \
  "v_mfma_f32_32x32x16_bf16 v[56:71], v[128:131], v[144:147], v[56:71]\n\t" \
  "ds_read_b128 v[128:131], v165 offset:14336\n\t" \
  "v_exp_f32 v38, v38\n\t" \
  "v_exp_f32 v39, v39\n\t" \
  "v_cvt_pk_bf16_f32 v148, v32, v33\n\t" \
  "v_cvt_pk_bf16_f32 v149, v34, v35\n\t" \
  "v_cvt_pk_bf16_f32 v150, v36, v37\n\t" \
  "v_cvt_pk_bf16_f32 v151, v38, v39\n\t" \
  "s_barrier\n\t" \
  "v_mfma_f32_32x32x16_bf16 v[24:39], v[120:123], v[112:115], v[88:103]\n\t" \
  "ds_read_b128 v[120:123], v166 offset:0\n\t" \
  "v_exp_f32 v8, v8\n\t" \
  "v_exp_f32 v9, v9\n\t" \
  "v_exp_f32 v10, v10\n\t" \
  "v_cvt_pk_bf16_f32 v136, v8, v9\n\t" \
  "v_mfma_f32_32x32x16_bf16 v[24:39], v[124:127], v[116:119], v[24:39]\n\t" \
  "ds_read_b128 v[124:127], v166 offset:1024\n\t" \
  "v_exp_f32 v11, v11\n\t" \
  "v_exp_f32 v12, v12\n\t" \
  "v_exp_f32 v13, v13\n\t" \
  "v_cvt_pk_bf16_f32 v137, v10, v11\n\t" \
  "v_smfmac_f32_16x16x64_bf16 v[156:159], v[160:163], v[144:151], v164\n\t" \
  "v_exp_f32 v14, v14\n\t" \
  "v_exp_f32 v15, v15\n\t" \
  "v_exp_f32 v16, v16\n\t" \
  "v_cvt_pk_bf16_f32 v138, v12, v13\n\t" \
  "v_mfma_f32_32x32x16_bf16 v[56:71], v[132:135], v[148:151], v[56:71]\n\t" \
  "ds_read_b128 v[132:135], v165 offset:15360\n\t" \
  "v_exp_f32 v17, v17\n\t" \
  "v_exp_f32 v18, v18\n\t" \
  "v_cvt_pk_bf16_f32 v139, v14, v15\n\t" \
  "v_exp_f32 v19, v19\n\t" \
  "v_exp_f32 v20, v20\n\t" \
  "v_exp_f32 v21, v21\n\t" \
  "s_waitcnt lgkmcnt(3)\n\t" \
  "v_mfma_f32_32x32x16_bf16 v[40:55], v[128:131], v[136:139], v[40:55]\n\t" \
  "v_exp_f32 v22, v22\n\t" \
  "v_exp_f32 v23, v23\n\t" \
  "v_cvt_pk_bf16_f32 v140, v16, v17\n\t" \
  "v_cvt_pk_bf16_f32 v141, v18, v19\n\t" \
  "v_cvt_pk_bf16_f32 v142, v20, v21\n\t" \
  "v_cvt_pk_bf16_f32 v143, v22, v23\n\t" \
  "s_waitcnt lgkmcnt(1)\n\t" \
  "v_mfma_f32_32x32x16_bf16 v[8:23], v[120:123], v[104:107], v[72:87]\n\t" \
  "v_exp_f32 v24, v24\n\t" \
  "v_exp_f32 v25, v25\n\t" \
  "v_exp_f32 v26, v26\n\t" \
  "v_cvt_pk_bf16_f32 v144, v24, v25\n\t" \
  "v_mfma_f32_32x32x16_bf16 v[8:23], v[124:127], v[108:111], v[8:23]\n\t" \
  "v_exp_f32 v27, v27\n\t" \
  "v_exp_f32 v28, v28\n\t" \
  "v_exp_f32 v29, v29\n\t" \
  "v_cvt_pk_bf16_f32 v145, v26, v27\n\t" \
  "v_smfmac_f32_16x16x64_bf16 v[152:155], v[160:163], v[136:143], v164\n\t" \
  "v_exp_f32 v30, v30\n\t" \
  "v_exp_f32 v31, v31\n\t" \
  "v_exp_f32 v32, v32\n\t" \
  "v_cvt_pk_bf16_f32 v146, v28, v29\n\t" \
  "s_waitcnt lgkmcnt(0)\n\t" \
  "v_mfma_f32_32x32x16_bf16 v[40:55], v[132:135], v[140:143], v[40:55]\n\t" \
  "v_exp_f32 v33, v33\n\t" \
  "v_exp_f32 v34, v34\n\t" \
  "v_cvt_pk_bf16_f32 v147, v30, v31\n\t" \
  "v_exp_f32 v35, v35\n\t" \
  "v_exp_f32 v36, v36\n\t" \
  "v_exp_f32 v37, v37\n\t" \
  "v_mfma_f32_32x32x16_bf16 v[56:71], v[128:131], v[144:147], v[56:71]\n\t" \
  "ds_read_b128 v[128:131], v166 offset:2048\n\t" \
  "v_exp_f32 v38, v38\n\t" \
  "v_exp_f32 v39, v39\n\t" \
  "v_cvt_pk_bf16_f32 v148, v32, v33\n\t" \
  "v_cvt_pk_bf16_f32 v149, v34, v35\n\t" \
  "v_cvt_pk_bf16_f32 v150, v36, v37\n\t" \
  "v_cvt_pk_bf16_f32 v151, v38, v39\n\t" \
  "v_mov_b32 v165, v166\n\t" \
  "s_add_u32 s48, s48, 0x8000\n\t" \
  "s_cmp_eq_u32 s48, 0x18000\n\t" \
  "s_cselect_b32 s48, 0, s48\n\t" \
  "v_add_u32 v166, s48, v167\n\t" \
  "s_add_u32 s52, s52, 1\n\t" \
  "s_cmp_lt_u32 s52, 8\n\t" \
  "s_cbranch_scc1 1b\n\t" \
  "v_smfmac_f32_16x16x64_bf16 v[156:159], v[160:163], v[144:151], v164\n\t" \
  "v_mfma_f32_32x32x16_bf16 v[56:71], v[132:135], v[148:151], v[56:71]\n\t" \
  "s_waitcnt lgkmcnt(0)\n\t"
#define AT_LOOP_CLOB "v8", "v9", "v10", "v11", "v12", "v13", "v14", "v15", "v16", "v17", "v18", "v19", "v20", "v21", "v22", "v23", "v24", "v25", "v26", "v27", "v28", "v29", "v30", "v31", "v32", "v33", "v34", "v35", "v36", "v37", "v38", "v39", "v120", "v121", "v122", "v123", "v124", "v125", "v126", "v127", "v128", "v129", "v130", "v131", "v136", "v137", "v138", "v139", "v140", "v141", "v142", "v143", "v165", "v166"

constexpr int AT_SLOT = 32768, AT_Q = 3 * AT_SLOT, AT_KVF = AT_Q + 16384, AT_EX = 2 * AT_SLOT;
DI float max3f(float a, float b, float c) { float r; asm("v_max3_f32 %0, %1, %2, %3" : "=v"(r) : "v"(a), "v"(b), "v"(c)); return r; }
#define MFMA_SETTLE(...) asm volatile("s_nop 11" : __VA_ARGS__)
DI float row_max(const f32x16& s) {
  const float a = max3f(max3f(max3f(s[0], s[1], s[2]), s[3], s[4]), s[5], s[6]), b = max3f(max3f(max3f(s[7], s[8], s[9]), s[10], s[11]), s[12], s[13]);
  const float m = max3f(max3f(a, s[14], s[15]), b, b);
  const auto r = __builtin_amdgcn_permlane32_swap(__float_as_uint(m), __float_as_uint(m), false, false);
  return max3f(__uint_as_float(r[0]), __uint_as_float(r[1]), __uint_as_float(r[1]));
}
DI void attn_slow(srd_t sws, unsigned kv, unsigned qp, unsigned op, int l) {
#define LDF(o) __builtin_bit_cast(bf16x8, ld16c(sws, l * 16, o))
  const bf16x8 q0 = LDF(qp), q1 = LDF(qp + 1024);
  f32x16 o = f32x16{}; float m = -INFINITY, sum = 0.f;
#pragma nounroll
  for (int t = 0; t < 64; ++t) {
    const unsigned p = kv + t * 4096;
    f32x16 s = MFMA32(LDF(p), q0, f32x16{}); s = MFMA32(LDF(p + 1024), q1, s);
    MFMA_SETTLE("+v"(s));
    const float mn = fmaxf(m, row_max(s)), a = __builtin_amdgcn_exp2f(m - mn); m = mn; float ps = 0.f;
#pragma unroll
    for (int r = 0; r < 16; ++r) { s[r] = rbf(__builtin_amdgcn_exp2f(s[r] - mn)); ps += s[r]; o[r] *= a; }
    sum = sum * a + ps;
    o = MFMA32(LDF(p + 2048), pack8(s, 0), o); o = MFMA32(LDF(p + 3072), pack8(s, 1), o);
  }
#undef LDF
  auto r = __builtin_amdgcn_permlane32_swap(__float_as_uint(sum), __float_as_uint(sum), false, false);
  o *= 1.f / (__uint_as_float(r[0]) + __uint_as_float(r[1]));
  st16c(sws, l * 16, op, pack8(o, 0)); st16c(sws, l * 16, op + 1024, pack8(o, 1));
}
DI void attn_compute(srd_t sws, int head, int qb, int wv, int l, LAS char* lds) {
  const int kh = wv >> 2, wq = wv & 3;
  f32x16 o[2] = {}; f32x4 ls[2] = {}, pz0 = {}, pz1 = {}, vz = {};
  const unsigned v = (l & 3) < 2 ? 0x3f803f80u : 0u, v0 = (l & 4) ? 0u : v, v1 = (l & 4) ? v : 0u;
  u32x4 sel = {v0, v0, v1, v1}; unsigned sidx = (l & 1) ? 0xEEEEEEEEu : 0x44444444u, base = (unsigned)(size_t)lds + kh * 16384 + l * 16;
  asm volatile("" : "+{v[160:163]}"(sel), "+{v164}"(sidx), "+{v167}"(base), "+{v[40:55]}"(o[0]), "+{v[56:71]}"(o[1]), "+{v[152:155]}"(ls[0]), "+{v[156:159]}"(ls[1]), "+{v[144:147]}"(pz0), "+{v[148:151]}"(pz1), "+{v[132:135]}"(vz));
  WAIT_LBAR();
  bf16x8 qf[2][2];
#pragma unroll
  for (int i = 0; i < 2; ++i) { const LAS char* p = lds + AT_Q + (wq * 2 + (i ? kh : 1 - kh)) * 2048 + l * 16; qf[i][0] = lds128(p); qf[i][1] = lds128(p + 1024); }
  f32x16 negm[2];
  { const bf16x8 k0 = lds128(lds + l * 16), k1 = lds128(lds + 1024 + l * 16);
    f32x16 s0 = MFMA32(k0, qf[0][0], f32x16{}), s1 = MFMA32(k0, qf[1][0], f32x16{}); s0 = MFMA32(k1, qf[0][1], s0); s1 = MFMA32(k1, qf[1][1], s1);
    MFMA_SETTLE("+v"(s0), "+v"(s1));
    const float m0 = -row_max(s0), m1 = -row_max(s1);
#pragma unroll
    for (int r = 0; r < 16; ++r) { negm[0][r] = m0; negm[1][r] = m1; } }
  asm volatile("" : "+{v[72:87]}"(negm[0]), "+{v[88:103]}"(negm[1]));
  WAIT_LBAR();
  asm volatile(AT_LOOP_ASM
    : "+{v[40:55]}"(o[0]), "+{v[56:71]}"(o[1]), "+{v[152:155]}"(ls[0]), "+{v[156:159]}"(ls[1]), "+{v[144:147]}"(pz0), "+{v[148:151]}"(pz1), "+{v[132:135]}"(vz)
    : "{v[72:87]}"(negm[0]), "{v[88:103]}"(negm[1]), "{v[104:107]}"(qf[0][0]), "{v[108:111]}"(qf[0][1]), "{v[112:115]}"(qf[1][0]), "{v[116:119]}"(qf[1][1]),
      "{v[160:163]}"(sel), "{v164}"(sidx), "{v167}"(base)
    : "memory", "scc", "s48", "s52", AT_LOOP_CLOB);
  l = lane_id();
  LAS f32x4* ex = (LAS f32x4*)(lds + AT_EX);
#pragma unroll
  for (int g = 0; g < 4; ++g) ex[(wv * 5 + g) * 64 + l] = f32x4{o[0][4 * g], o[0][4 * g + 1], o[0][4 * g + 2], o[0][4 * g + 3]};
  ((LAS float*)&ex[(wv * 5 + 4) * 64])[l] = ls[0][0] + ls[0][1];
  MFMA_SETTLE("+v"(o[1]), "+v"(ls[1]));
  WAIT_LBAR();
  f32x16 of;
#pragma unroll
  for (int g = 0; g < 4; ++g) { const f32x4 a = ex[((wv ^ 4) * 5 + g) * 64 + l];
#pragma unroll
    for (int e = 0; e < 4; ++e) of[4 * g + e] = o[1][4 * g + e] + a[e]; }
  const float lf = ls[1][0] + ls[1][1] + ((const LAS float*)&ex[((wv ^ 4) * 5 + 4) * 64])[l];
  const unsigned tile = qb * 8 + wq * 2 + kh, op = WS_OB + (tile * 32 + head) * 2048;
  of *= __builtin_amdgcn_rcpf(lf);
  st16c(sws, l * 16, op, pack8(of, 0)); st16c(sws, l * 16, op + 1024, pack8(of, 1));
  f32x4 chk = {};
#pragma unroll
  for (int r = 0; r < 16; ++r) chk[r & 3] = __builtin_fmaf(of[r], 0.f, chk[r & 3]);
  if (!__all(lf < 0x1p100f && (chk[0] + chk[1]) + (chk[2] + chk[3]) == 0.f)) attn_slow(sws, WS_KV + head * 262144, WS_QB + (head * 64 + tile) * 2048, op, l);
}
DI void attn_service(char* ws, srd_t sws, u64 tag, bool bra, int rb1, int cb1, srd_t swo, int head, int qb, int w4, int l, LAS char* lds) {
  const unsigned lds0 = (unsigned)(size_t)lds, vl = l * 16, fb = WS_F1 + (16 + (head >> 1)) * 256; volatile LAS unsigned* word = (volatile LAS unsigned*)(lds + LDS_WORD);
#define AT_DMA(j) do { _Pragma("unroll") for (int p = 0; p < 8; ++p) \
    dma16c(sws, vl, WS_KV + head * 262144 + (8 * (j) + (p >> 2) * 4 + w4) * 4096 + (p & 3) * 1024, lds0 + ((j) % 3) * AT_SLOT + (p >> 2) * 16384 + w4 * 4096 + (p & 3) * 1024); } while (0)
  wait_tags(ws, fb, 2, qb, tag, w4, l, word, 1);
#pragma unroll
  for (int p = 0; p < 4; ++p) dma16c(sws, vl, WS_QB + (head * 64 + qb * 8 + w4 * 2) * 2048 + p * 1024, lds0 + AT_Q + w4 * 4096 + p * 1024);
  AT_DMA(0);
  WAIT_BAR(6);
  WAIT_BAR(0);
  wait_tags(ws, fb + 8, 1, 0, tag, w4, l, word, 2);
  AT_DMA(1);
  if (bra) {
    const LAS f32x4* red = (const LAS f32x4*)(lds + LDS_KVP) + w4 * 64 + l; const LAS f32x4* rz = (const LAS f32x4*)(lds + LDS_ZP) + (w4 & 1) * 8 + (l & 7);
#pragma unroll
    for (int hh = 0; hh < 2; ++hh) st16c(sws, vl, WS_KVP + (((2 * cb1 + hh) * 8 + rb1) * 4 + w4) * 1024, (red[hh * 256] + red[(2 + hh) * 256]) + (red[(4 + hh) * 256] + red[(6 + hh) * 256]));
    if (w4 < 2 && l < 8) st16c(sws, vl, WS_ZP + ((2 * cb1 + w4) * 8 + rb1) * 128, (rz[0] + rz[16]) + (rz[32] + rz[48]));
  }
  wait_tags(ws, fb, 8, 1, tag, w4, l, word, 3);
  WAIT_BAR(0);
  AT_DMA(2);
  if (bra) {
#pragma unroll
    for (int p = 0; p < 4; ++p) st16c(sws, vl, WS_PQ + (cb1 * 64 + rb1 * 8 + w4 * 2) * 2048 + p * 1024, lds128(lds + LDS_PQ + 16384 + w4 * 4096 + p * 1024 + vl));
  }
#define AT_WARM(i) warm4(swo, (unsigned)((blockIdx.x >> 3) * 32 + w4 * 8 + (i) * 2 + ((l >> 3) & 1)) * 2048u + (blockIdx.x & 1) * 1024 + (l & 7) * 128, 0u, lds0 + LDS_DUMMY)
  AT_WARM(0);
  WAIT_BAR(1);
  if (bra && w4 == 0 && l == 0) put_tag(ws, WS_F1 + cb1 * 256 + rb1 * 8, tag);
  AT_DMA(3);
  AT_WARM(1);
  WAIT_BAR(1);
  AT_DMA(4);
  AT_WARM(2);
  WAIT_BAR(1);
  AT_DMA(5);
  const unsigned ha = 2 * cb1 + !bra;
  wait_tags(ws, WS_F1 + cb1 * 256, 8, 1, tag, w4, l, word, 4);
  { typedef unsigned u32x2 __attribute__((ext_vector_type(2)));
    f32x4 s = __builtin_bit_cast(f32x4, ld16c(sws, vl, WS_KVP + ha * 32768 + w4 * 1024));
#pragma unroll
    for (int r = 1; r < 8; ++r) s += __builtin_bit_cast(f32x4, ld16c(sws, vl, WS_KVP + ha * 32768 + r * 4096 + w4 * 1024));
    *(LAS u32x2*)(lds + AT_KVF + ((l >> 4) * 32 + (w4 >> 1) * 16 + (l & 15)) * 16 + (w4 & 1) * 8) = u32x2{cvtpk(s[0], s[1]), cvtpk(s[2], s[3])};
    if (w4 == 0) { f32x4 z = __builtin_bit_cast(f32x4, ld16c(sws, vl, WS_ZP + ha * 1024));
#pragma unroll
      for (int e = 0; e < 4; ++e) { z[e] += __shfl_xor(z[e], 8); z[e] += __shfl_xor(z[e], 16); z[e] += __shfl_xor(z[e], 32); }
      if (l < 8) *(LAS u32x2*)(lds + AT_KVF + 2048 + (l & 3) * 16 + (l >> 2) * 8) = u32x2{cvtpk(z[0], z[1]), cvtpk(z[2], z[3])}; }
    if (!bra) {
#pragma unroll
      for (int p = 0; p < 4; ++p) dma16c(sws, vl, WS_PQ + (cb1 * 64 + rb1 * 8 + w4 * 2) * 2048 + p * 1024, lds0 + LDS_PQ + w4 * 4096 + p * 1024); } }
  WAIT_BAR(0);
  AT_DMA(6);
  { const bf16x8 kv0 = lds128(lds + AT_KVF + vl), kv1 = lds128(lds + AT_KVF + 1024 + vl), z0 = lds128(lds + AT_KVF + 2048 + (l >> 5) * 16), z1 = lds128(lds + AT_KVF + 2080 + (l >> 5) * 16);
#pragma unroll
    for (int i = 0; i < 2; ++i) { const unsigned op = WS_OA + ((rb1 * 8 + w4 * 2 + i) * 32 + ha) * 2048;
      const bf16x8 pq0 = lds128(lds + LDS_PQ + (w4 * 2 + i) * 2048 + vl), pq1 = lds128(lds + LDS_PQ + (w4 * 2 + i) * 2048 + 1024 + vl);
      f32x16 num = MFMA32(kv0, pq0, f32x16{}); num = MFMA32(kv1, pq1, num);
      f32x16 den = MFMA32(z0, pq0, f32x16{}); den = MFMA32(z1, pq1, den);
      num *= __builtin_amdgcn_rcpf(den[0] + 1e-6f);
      st16c(sws, vl, op, pack8(num, 0)); st16c(sws, vl, op + 1024, pack8(num, 1)); } }
  WAIT_BAR(0);
  if (w4 == 0 && l == 0) put_tag(ws, WS_F2 + (rb1 * 32 + ha) * T2, tag);
  AT_DMA(7);
  if (blockIdx.x == 0 && w4 == 0) {
    const GAS u64* p = (const GAS u64*)(ws + WS_F1 + (l >> 3) * 256) + (l & 7); const u64 t0 = __builtin_amdgcn_s_memrealtime();
#define LDT(i) __hip_atomic_load(p + (i) * 256, __ATOMIC_RELAXED, __HIP_MEMORY_SCOPE_AGENT)
    for (;;) { const u64 a = LDT(0), b = LDT(1), c = LDT(2), d = LDT(3);
      if (__all(a == tag && b == tag && c == tag && d == tag) || __builtin_amdgcn_s_memrealtime() - t0 > 2000000ull) break; }
#undef LDT
    if (l == 0) put_tag(ws, WS_EP, tag);
  }
  AT_WARM(3);
#undef AT_WARM
  WAIT_BAR(1); BAR();
  BAR();
#undef AT_DMA
}

constexpr int G3_SLOT = 32768;
DI void out_service(char* ws, srd_t sws, u64 tag, srd_t sw, int br, int rb, int cb, int w4, int l, LAS char* lds) {
  const unsigned lds0 = (unsigned)(size_t)lds;
  const unsigned vo = l * 16 + (br ? WS_OB : WS_OA) + (rb * 4 + w4) * 65536;
  const unsigned vw = (unsigned)(w4 * 16 + (l >> 3)) * 2048u + cb * 256 + ((((l >> 1) & 3) ^ (l >> 4)) * 32) + (l & 1) * 16;
#define G3_O(kt) do { _Pragma("unroll") for (int p = 0; p < 4; ++p) dma16c(sws, vo, (kt) * 4096u + p * 1024u, lds0 + ((kt) & 3) * G3_SLOT + w4 * 4096 + p * 1024); } while (0)
#define G3_W(kt) do { _Pragma("unroll") for (int p = 0; p < 4; ++p) \
    dma16(sw, vw, (kt) * (64u * 2048u) + (p & 1) * (8u * 2048u) + (p >> 1) * 128u, lds0 + ((kt) & 3) * G3_SLOT + 16384 + (p >> 1) * 8192 + w4 * 2048 + (p & 1) * 1024); } while (0)
#pragma nounroll
  for (int kt = 0; kt < 4; ++kt) G3_W(kt);
  wait_tags(ws, WS_F2 + (br * 8 + (rb >> 1)) * 32 * T2, 32, T2 / 8, tag, w4, l, (volatile LAS unsigned*)(lds + LDS_WORD), 5);
#pragma nounroll
  for (int kt = 0; kt < 4; ++kt) G3_O(kt);
  WAIT_BAR(12);
  WAIT_BAR(8); G3_O(4); G3_W(4);
  WAIT_BAR(12); G3_O(5); G3_W(5);
#pragma nounroll
  for (int kt = 2; kt < 12; ++kt) { WAIT_BAR(16); G3_O(kt + 4); G3_W(kt + 4); }
  WAIT_BAR(16); WAIT_BAR(8); WAIT_BAR(0);
#undef G3_O
#undef G3_W
}
DI void out_pair(u16* p, const f32x4& u, const f32x4& v) {
  const auto lo = __builtin_amdgcn_permlane16_swap(cvtpk(u[0], u[1]), cvtpk(v[0], v[1]), false, false), hi = __builtin_amdgcn_permlane16_swap(cvtpk(u[2], u[3]), cvtpk(v[2], v[3]), false, false);
  st_out(p, u32x4{lo[0], hi[0], lo[1], hi[1]});
}
DI void out_compute(u16* out, int wv, int l, LAS char* lds) {
  const int kg = wv >> 2, wm = (wv >> 1) & 1, wn = wv & 1, c = l & 15, g = l >> 4;
  const LAS char* const ao[2] = {lds + wm * 8192 + (1 - kg) * 4096 + kg * 2048 + g * 512 + c * 16, lds + wm * 8192 + kg * 4096 + kg * 2048 + g * 512 + c * 16};
  const LAS char* aw[4];
#pragma unroll
  for (int j = 0; j < 4; ++j) aw[j] = lds + 16384 + wn * 8192 + (32 * kg + 16 * (g >> 1) + 4 * (g & 1) + (c >> 2)) * 128 + ((j ^ (2 * (g & 1) + (c >> 3))) * 32) + (c & 3) * 8;
  f32x4 acc[4][4]; bf16x8 fo[2][4]; s16x4 fw[2][4][2];
#define G3_RO(b, so_, i) fo[b][i] = lds128(ao[(i) >> 1] + (so_) + ((i) & 1) * 256)
#define G3_RW(b, so_, j, t) fw[b][j][t] = __builtin_amdgcn_ds_read_tr16_b64_v4i16((LAS s16x4*)(aw[j] + (so_) + (t) * 1024))
#define G3_MM1(Z, b, i, j) acc[i][j] = MFMA16(__builtin_bit_cast(bf16x8, __builtin_shufflevector(fw[b][j][0], fw[b][j][1], 0, 1, 2, 3, 4, 5, 6, 7)), fo[b][i], (Z) ? f32x4{} : acc[i][j])
#define G3_MM(Z, b, i, j) G3_MM1(Z, b, ((j) & 1) ? 3 - (i) : (i), j)
#define G3_GRP(Z, b, j, R0, R1, R2, R3) do { G3_MM(Z, b, 0, j); SBAR(); R0; SBAR(); G3_MM(Z, b, 1, j); SBAR(); R1; SBAR(); G3_MM(Z, b, 2, j); SBAR(); R2; SBAR(); G3_MM(Z, b, 3, j); SBAR(); R3; SBAR(); } while (0)
#define G3_STEP(Z, b, RD, so_) do { SBAR(); \
    G3_GRP(Z, b, 0, , , , ); if (RD) WAIT_LBAR(); SBAR(); \
    G3_GRP(Z, b, 1, if (RD) G3_RW(b ^ 1, so_, 0, 0), if (RD) G3_RW(b ^ 1, so_, 0, 1), if (RD) G3_RO(b ^ 1, so_, 0), if (RD) G3_RO(b ^ 1, so_, 1)); \
    G3_GRP(Z, b, 2, if (RD) G3_RO(b ^ 1, so_, 2), if (RD) G3_RO(b ^ 1, so_, 3), if (RD) G3_RW(b ^ 1, so_, 1, 0), if (RD) G3_RW(b ^ 1, so_, 1, 1)); \
    G3_GRP(Z, b, 3, if (RD) G3_RW(b ^ 1, so_, 2, 0), if (RD) G3_RW(b ^ 1, so_, 2, 1), if (RD) G3_RW(b ^ 1, so_, 3, 0), if (RD) G3_RW(b ^ 1, so_, 3, 1)); } while (0)
  BAR();
  G3_RW(0, 0, 0, 0); G3_RW(0, 0, 0, 1);
#pragma unroll
  for (int i = 0; i < 4; ++i) G3_RO(0, 0, i);
#pragma unroll
  for (int j = 1; j < 4; ++j) { G3_RW(0, 0, j, 0); G3_RW(0, 0, j, 1); }
  G3_STEP(true, 0, true, G3_SLOT);
#pragma nounroll
  for (int kt = 1; kt < 13; kt += 2) { G3_STEP(false, 1, true, ((kt + 1) & 3) * G3_SLOT); G3_STEP(false, 0, true, ((kt + 2) & 3) * G3_SLOT); }
  G3_STEP(false, 1, true, 2 * G3_SLOT);
  LAS f32x4* ex = (LAS f32x4*)lds + wv * 256 + l; const LAS f32x4* ey = (const LAS f32x4*)lds + (wv ^ 4) * 256 + l;
  u16* op = out + (size_t)(wm * 64 + kg * 32 + c) * 2048 + wn * 64 + (g & 1) * 16 + (g >> 1) * 8;
  f32x4 pr[4];
#define G3_M(b, i, j, R) do { G3_MM1(false, b, i, j); SBAR(); R; SBAR(); } while (0)
#define G3_GK(b, j, R0, R1, R2, R3) do { G3_M(b, 0, j, R0); G3_M(b, 1, j, R1); G3_M(b, 2, j, R2); G3_M(b, 3, j, R3); } while (0)
#define G3_GIVE(jp, k) ex[(jp) * 2048 + (k) * 64] = acc[(k) >> 1][2 * (jp) + ((k) & 1)]
#define G3_TAKE(jp, k) pr[k] = ey[(jp) * 2048 + (k) * 64]
#define G3_OUT(jp, i) out_pair(op + (size_t)(i) * 16 * 2048 + (jp) * 32, acc[2 + (i)][2 * (jp)] + pr[2 * (i)], acc[2 + (i)][2 * (jp) + 1] + pr[2 * (i) + 1])
  SBAR();
  G3_GK(0, 0, , , , ); WAIT_LBAR(); SBAR();
  G3_GK(0, 1, G3_RW(1, 3 * G3_SLOT, 0, 0), G3_RW(1, 3 * G3_SLOT, 0, 1), G3_RO(1, 3 * G3_SLOT, 0), G3_RO(1, 3 * G3_SLOT, 1));
  G3_GK(0, 2, G3_RO(1, 3 * G3_SLOT, 2), G3_RO(1, 3 * G3_SLOT, 3), G3_RW(1, 3 * G3_SLOT, 1, 0), G3_RW(1, 3 * G3_SLOT, 1, 1));
  G3_GK(1, 0, G3_RW(1, 3 * G3_SLOT, 2, 0), G3_RW(1, 3 * G3_SLOT, 2, 1), G3_RW(1, 3 * G3_SLOT, 3, 0), G3_RW(1, 3 * G3_SLOT, 3, 1));
  G3_GK(1, 1, G3_GIVE(0, 0), G3_GIVE(0, 2), , );
  G3_GK(0, 3, G3_GIVE(0, 1), G3_GIVE(0, 3), , );
  WAIT_LBAR(); SBAR();
  G3_GK(1, 2, G3_TAKE(0, 0); G3_TAKE(0, 1), G3_TAKE(0, 2); G3_TAKE(0, 3), , );
  G3_GK(1, 3, G3_GIVE(1, 0), G3_GIVE(1, 2), G3_OUT(0, 0), G3_GIVE(1, 1); G3_GIVE(1, 3));
  G3_OUT(0, 1);
  WAIT_LBAR();
  G3_TAKE(1, 0); G3_TAKE(1, 1); G3_TAKE(1, 2); G3_TAKE(1, 3); SBAR();
  G3_OUT(1, 0); G3_OUT(1, 1);
#undef G3_M
#undef G3_GK
#undef G3_GIVE
#undef G3_TAKE
#undef G3_OUT
#undef G3_RO
#undef G3_RW
#undef G3_MM
#undef G3_MM1
#undef G3_GRP
#undef G3_STEP
}

extern "C" __global__ __launch_bounds__(768, 3) void fused_kernel(const u16* xa, const u16* wa, const u16* woa, const u16* xb, const u16* wb, const u16* wob, char* ws, u16* out) {
  extern __shared__ __attribute__((aligned(16))) char smem[];
  LAS char* lds = (LAS char*)smem;
  const int l = threadIdx.x & 63, wv = rfl(threadIdx.x >> 6);
  const int xcd = blockIdx.x & 7, idx = blockIdx.x >> 3;
  const int rb1 = (xcd >> 2) * 4 + (idx & 3), cb1 = (xcd & 3) * 4 + ((idx >> 2) & 3); const bool bra = idx >= 16;
  const int head = (2 * ((xcd + 1) & 3) + (xcd >> 2)) * 4 + (idx & 3), qb = idx >> 2;
  const int br3 = xcd >> 2, rb3 = ((xcd >> 1) & 1) * 8 + (idx & 7), cb3 = (xcd & 1) * 4 + (idx >> 3);
  if (__builtin_expect(wv < 8, 0)) {
    if (wv < 4) WARM_CODE(idx * 4 + wv, "");
    const QkvAddr q = qkv_setup(bra ? wa : wb, rb1, cb1, wv, l, (unsigned)(size_t)lds);
    BAR();
    const srd_t sws = mk_srd(ws, WS_END);
    if (bra) qkv_compute<true>(ws, sws, q, rb1, cb1, wv, lds); else qkv_compute<false>(ws, sws, q, rb1, cb1, wv, lds);
    attn_compute(sws, head, qb, wv, lane_id(), lds);
    WAIT_BAR(0);
    out_compute(out + (size_t)rb3 * 128 * 2048 + br3 * 1024 + cb3 * 128, wv, lane_id(), lds);
    return;
  }
  {
    const int w4 = wv - 8; const unsigned lds0 = (unsigned)(size_t)lds;
    __builtin_amdgcn_s_setprio(3);
    const u64 tag = qkv_stage(ws, xa, wa, xb, wb, bra, rb1, cb1, w4, l, lds0);
    const srd_t sws = mk_srd(ws, WS_END);
    if (w4 == 0) *(volatile LAS u64*)(lds + LDS_WORD + 8) = tag;
    WAIT_LBAR();
    const srd_t swo = mk_srd(br3 ? wob : woa, DM * DM * 2);
    attn_service(ws, sws, tag, bra, rb1, cb1, swo, head, qb, w4, l, lds);
    BAR();
    if (w4 == 0 && l == 0) put_tag(ws, WS_F2 + ((8 + qb) * 32 + head) * T2, tag);
    out_service(ws, sws, tag, swo, br3, rb3, cb3, w4, l, lds);
    BAR(); BAR();
  }
}
extern "C" __device__ __attribute__((used, noinline, visibility("hidden"))) void code_end_mark() {}

extern "C" void kernel_launch(void* const* d_in, const int* in_sizes, int n_in, void* d_out, int out_size, void* d_ws, size_t ws_size, hipStream_t stream) {
  (void)hipFuncSetAttribute((const void*)fused_kernel, hipFuncAttributeMaxDynamicSharedMemorySize, LDS_BYTES);
  fused_kernel<<<256, 768, LDS_BYTES, stream>>>((const u16*)d_in[0], (const u16*)d_in[1], (const u16*)d_in[2], (const u16*)d_in[3], (const u16*)d_in[4], (const u16*)d_in[5],
                                                (char*)d_ws, (u16*)d_out);
}
```
